```python
import torch

def _softmax(x, axis=-1):
    return torch.softmax(x, dim=axis)
def _gelu(x):
    return torch.nn.functional.gelu(x, approximate="tanh")
def _silu(x):
    return torch.nn.functional.silu(x)
def _sigmoid(x):
    return torch.sigmoid(x)
def _layernorm(x, axis=-1, eps=1e-5):
    m = x.mean(dim=axis, keepdim=True)
    v = x.var(dim=axis, keepdim=True, unbiased=False)
    return (x - m) / torch.sqrt(v + eps)
def _scatter_add(tgt, idx, src, axis=0):
    return tgt.clone().index_add_(axis, idx, src)
def _segment_sum(src, seg, n):
    out = torch.zeros((n,) + src.shape[1:], dtype=src.dtype)
    return out.index_add_(0, seg, src)
def _pad(x, pw):
    flat = tuple(v for pair in reversed(pw) for v in pair)
    return torch.nn.functional.pad(x, flat)

kernel_name = 'gated_mix__ssm_conv_s4d__mixer_token_mlp__prompt'

def setup_inputs():

    _w = (lambda a: (lambda t: t.to(torch.bfloat16) if t.is_floating_point() else t)(torch.as_tensor(a)))
    def _A_setup():
        Bsz, L, D, N = 1, 2048, 576, 128
        return {
            "u": _w((lambda *s: torch.randn(*s, dtype=torch.bfloat16) * 0.05)(Bsz, L, D)),
            "log_A_real": _w((lambda *s: torch.randn(*s, dtype=torch.bfloat16) * 3.2)(D, N) * 0.5),
            "A_imag": _w((lambda *s: torch.randn(*s, dtype=torch.bfloat16) * 0.05)(D, N) * 3.14),
            "C_real": _w((lambda *s: torch.randn(*s, dtype=torch.bfloat16) * 0.1)(D, N) / N ** 0.5),
            "C_imag": _w((lambda *s: torch.randn(*s, dtype=torch.bfloat16) * 0.1)(D, N) / N ** 0.5),
            "log_dt": _w((lambda *s: torch.randn(*s, dtype=torch.bfloat16) * 0.05)(D) * 0.5 - 4.0),
            "D_skip": _w((lambda *s: torch.randn(*s, dtype=torch.bfloat16) * 0.05)(D)),
        }
    _A_in = _A_setup()

    def _B_setup():
        B, S, D, F = 1, 2048, 576, 4608
        return {
            "x": _w((lambda *s: torch.randn(*s, dtype=torch.bfloat16) * 0.05)(B, S, D)),
            "w1": _w((lambda *s: torch.randn(*s, dtype=torch.bfloat16) * 0.2)(S, F) * (S ** -0.5)),
            "b1": _w((lambda *s: torch.randn(*s, dtype=torch.bfloat16) * 3.2)(F) * 0.02),
            "w2": _w((lambda *s: torch.randn(*s, dtype=torch.bfloat16) * 0.05)(F, S) * (F ** -0.5)),
            "b2": _w((lambda *s: torch.randn(*s, dtype=torch.bfloat16) * 0.1)(S) * 0.02),
        }
    _B_in = _B_setup()

    return {
        "A_u": _A_in["u"],
        "A_log_A_real": _A_in["log_A_real"],
        "A_A_imag": _A_in["A_imag"],
        "A_C_real": _A_in["C_real"],
        "A_C_imag": _A_in["C_imag"],
        "A_log_dt": _A_in["log_dt"],
        "A_D_skip": _A_in["D_skip"],
        "B_x": _B_in["x"],
        "B_w1": _B_in["w1"],
        "B_b1": _B_in["b1"],
        "B_w2": _B_in["w2"],
        "B_b2": _B_in["b2"],
        "gate_w": _w((lambda *s: torch.randn(*s, dtype=torch.bfloat16) * 0.05)(576) * 16.0),
    }

def reference(A_u, A_log_A_real, A_A_imag, A_C_real, A_C_imag, A_log_dt, A_D_skip, B_x, B_w1, B_b1, B_w2, B_b2, gate_w):
    def _A_ref(u, log_A_real, A_imag, C_real, C_imag, log_dt, D_skip):
        L = u.shape[1]
        dt = torch.exp(log_dt)
        A = -torch.exp(log_A_real) + 1j * A_imag
        C = (C_real + 1j * C_imag) * dt[:, None]
        pos = (lambda x, dt: x.to(dt))(torch.arange(L), log_dt.dtype)
        P = torch.exp((A * dt[:, None])[:, :, None] * pos[None, None, :])
        K = 2.0 * torch.einsum("dn,dnl->dl", C, P).real
        L2 = 2 * L
        u_t = (lambda x, *d: x.permute(*d))(u, 0, 2, 1)
        Kf = (lambda x, *a, **k: torch.fft.rfft(x.float(), *a, **k))(K, L2)
        Uf = (lambda x, *a, **k: torch.fft.rfft(x.float(), *a, **k))(u_t, L2)
        y = (lambda x, *a, **k: torch.fft.irfft(x, *a, **k).to(torch.bfloat16))(Uf * Kf[None, :, :], L2)[:, :, :L]
        y = (lambda x, *d: x.permute(*d))(y, 0, 2, 1)
        return y + u * D_skip[None, None, :]

    def _B_ref(x, w1, b1, w2, b2):
        B, S, D = x.shape
        h = _layernorm(x, axis=-1)
        u = torch.einsum("bsd,sf->bdf", h, w1) + b1
        u = _gelu(u)
        y = torch.einsum("bdf,fs->bds", u, w2) + b2
        return (lambda x, *d: x.permute(*d))(y, 0, 2, 1)

    x = A_u
    g = _sigmoid((x * gate_w).sum(-1, keepdims=True))
    a = _A_ref(x, A_log_A_real, A_A_imag, A_C_real, A_C_imag, A_log_dt, A_D_skip)
    b = _B_ref(x, B_w1, B_b1, B_w2, B_b2)
    return g * a + (1 - g) * b
```

```cpp
#include <hip/hip_runtime.h>
#include <hip/hip_bf16.h>
#include <stdint.h>

typedef unsigned short bf16_t;
typedef short bf16x8 __attribute__((ext_vector_type(8)));
typedef short bf16x4 __attribute__((ext_vector_type(4)));
typedef float f32x4 __attribute__((ext_vector_type(4)));
typedef unsigned u32x4 __attribute__((ext_vector_type(4)));
typedef unsigned u32x2 __attribute__((ext_vector_type(2)));
#define LAS __attribute__((address_space(3)))

constexpr int L_ = 2048, D_ = 576, NM_ = 128, F_ = 4608;

__device__ __forceinline__ float bf2f(bf16_t v) { return __uint_as_float(((unsigned)v) << 16); }
__device__ __forceinline__ bf16_t f2bf(float f) {
  __hip_bfloat16 h = __float2bfloat16(f);
  return __builtin_bit_cast(unsigned short, h);
}
__device__ __forceinline__ float rbf(float f) { return bf2f(f2bf(f)); }
__device__ __forceinline__ unsigned pack2(float lo, float hi) { return (unsigned)f2bf(lo) | ((unsigned)f2bf(hi) << 16); }

constexpr size_t WS_HT = 0;
constexpr size_t WS_AT = WS_HT + (size_t)D_ * L_ * 2;
constexpr size_t WS_G  = WS_AT + (size_t)D_ * L_ * 2;
constexpr size_t WS_U  = WS_G + (size_t)L_ * 4;
constexpr size_t WS_P  = WS_U + (size_t)D_ * F_ * 2;
constexpr size_t WS_END = WS_P + (size_t)4 * L_ * D_ * 4;

constexpr int LN_ROWS = 32, LN_WGS = L_ / LN_ROWS;
constexpr int LN_STRIDE = 1152 + 32;

__device__ __forceinline__ void ln_block(const bf16_t* __restrict__ x, const bf16_t* __restrict__ gate_w,
                                         bf16_t* __restrict__ hT, float* __restrict__ gate, int blk, LAS char* smem) {
  const int tid = threadIdx.x, wid = tid >> 6, lane = tid & 63;
  const int r = lane >> 3, c = lane & 7;
  const int srow = wid * 8 + r;
  const int s = blk * LN_ROWS + srow;
  float xv[72];
  float sum = 0.f, gs = 0.f;
#pragma unroll
  for (int i = 0; i < 9; ++i) {
    const u32x4 v = *(const u32x4*)(x + (size_t)s * D_ + (c + 8 * i) * 8);
    const u32x4 w = *(const u32x4*)(gate_w + (c + 8 * i) * 8);
#pragma unroll
    for (int j = 0; j < 4; ++j) {
      const float a0 = __uint_as_float(v[j] << 16), a1 = __uint_as_float(v[j] & 0xffff0000u);
      const float w0 = __uint_as_float(w[j] << 16), w1 = __uint_as_float(w[j] & 0xffff0000u);
      xv[i * 8 + 2 * j] = a0; xv[i * 8 + 2 * j + 1] = a1;
      sum += a0 + a1;
      gs += rbf(a0 * w0) + rbf(a1 * w1);
    }
  }
  sum += __shfl_xor(sum, 1); sum += __shfl_xor(sum, 2); sum += __shfl_xor(sum, 4);
  gs += __shfl_xor(gs, 1); gs += __shfl_xor(gs, 2); gs += __shfl_xor(gs, 4);
  const float mean = sum * (1.0f / D_);
  float vs = 0.f;
#pragma unroll
  for (int i = 0; i < 72; ++i) { const float d = xv[i] - mean; vs += d * d; }
  vs += __shfl_xor(vs, 1); vs += __shfl_xor(vs, 2); vs += __shfl_xor(vs, 4);
  const float mb = rbf(mean);
  const float vb = rbf(vs * (1.0f / D_));
  const float den = rbf(sqrtf(rbf(vb + 1e-5f)));
  if (c == 0) {
    const float z = rbf(gs);
    gate[s] = rbf(1.0f / (1.0f + expf(-z)));
  }
#pragma unroll
  for (int i = 0; i < 9; ++i) {
    u32x4 o;
#pragma unroll
    for (int j = 0; j < 4; ++j)
      o[j] = pack2(rbf(xv[i * 8 + 2 * j] - mb) / den, rbf(xv[i * 8 + 2 * j + 1] - mb) / den);
    *(LAS u32x4*)(smem + srow * LN_STRIDE + (c + 8 * i) * 16) = o;
  }
  __syncthreads();
  const int g = lane >> 4, q = (lane & 15) >> 2, p = lane & 3;
  for (int db = wid; db < D_ / 16; db += 4) {
    const LAS char* a0 = smem + (8 * g + q) * LN_STRIDE + db * 32 + p * 8;
    const bf16x4 v0 = __builtin_amdgcn_ds_read_tr16_b64_v4i16((LAS bf16x4*)a0);
    const bf16x4 v1 = __builtin_amdgcn_ds_read_tr16_b64_v4i16((LAS bf16x4*)(a0 + 4 * LN_STRIDE));
    bf16x8 o = __builtin_shufflevector(v0, v1, 0, 1, 2, 3, 4, 5, 6, 7);
    *(bf16x8*)(hT + (size_t)(db * 16 + (lane & 15)) * L_ + blk * LN_ROWS + 8 * g) = o;
  }
}

constexpr int SS_PAR = 0;
constexpr int SS_PARL = SS_PAR + 2048;
constexpr int SS_KQ = SS_PARL + 512;
constexpr int SS_KH = SS_KQ + 3072;
constexpr int SS_KL = SS_KH + 4608;
constexpr int SS_UL = SS_KL + 4608;
constexpr int SS_V = SS_UL + 4224;
constexpr int SS_VSTR = 4160;
constexpr int SS_END = SS_V + 8 * SS_VSTR;

__device__ __forceinline__ int prow_of(int r) {
  if (r < 16) return 128 * (r >> 3) + (r & 7);
  if (r < 24) return 256 + 128 * ((r - 16) >> 2) + 2 * ((r - 16) & 3);
  if (r < 32) return 512 + 128 * ((r - 24) >> 1) + 4 * ((r - 24) & 1);
  if (r <= 40) return 1024 + 128 * (r - 32);
  return 0;
}
__device__ __forceinline__ int kq_index(int l) {
  const int pos = (int)rbf((float)l);
  if (pos < 256) return ((pos >> 7) * 8 + (pos & 7)) * 16 + ((pos >> 3) & 15);
  if (pos < 512) { const int q = pos - 256; return (16 + (q >> 7) * 4 + ((q & 7) >> 1)) * 16 + ((q >> 3) & 15); }
  if (pos < 1024) { const int q = pos - 512; return (24 + (q >> 7) * 2 + ((q & 7) >> 2)) * 16 + ((q >> 3) & 15); }
  if (pos < 2048) { const int q = pos - 1024; return (32 + (q >> 7)) * 16 + ((q >> 3) & 15); }
  return 40 * 16;
}
__device__ __forceinline__ void cexp_p(float are2, float aih, float ail, float p, float& er, float& ei) {
  const float mag = __builtin_amdgcn_exp2f(are2 * p);
  const float pr = aih * p;
  const float er2 = __builtin_fmaf(aih, p, -pr);
  const float rr = __builtin_amdgcn_fractf(pr) + __builtin_fmaf(ail, p, er2);
  er = mag * __builtin_amdgcn_cosf(rr);
  ei = mag * __builtin_amdgcn_sinf(rr);
}

__device__ __forceinline__ void ssm_block(const bf16_t* __restrict__ x, const bf16_t* __restrict__ logA, const bf16_t* __restrict__ Aim,
                                          const bf16_t* __restrict__ Cre, const bf16_t* __restrict__ Cim, const bf16_t* __restrict__ logdt,
                                          const bf16_t* __restrict__ Dskip, bf16_t* __restrict__ aT, int d, LAS char* smem) {
  const int tid = threadIdx.x, wid = tid >> 6, lane = tid & 63;
  bf16_t ucol[8];
#pragma unroll
  for (int k = 0; k < 8; ++k) ucol[k] = x[(size_t)(tid + 256 * k) * D_ + d];
  const float dt = rbf(expf(bf2f(logdt[d])));
  if (tid < NM_) {
    const float are = -rbf(expf(bf2f(logA[d * NM_ + tid]))) * dt;
    const float aim = bf2f(Aim[d * NM_ + tid]) * dt;
    const float i2pi_h = 0.15915494f, i2pi_l = 6.4206383e-09f + (0.15915494309189535 - (double)0.15915494f > 0 ? 0.f : 0.f);
    const double dummy = 0; (void)dummy; (void)i2pi_l;
    const float I2H = 0.159154936671257019043f;
    const float I2L = 6.42063824376987e-09f;
    (void)i2pi_h;
    const float aih = aim * I2H;
    const float ail = __builtin_fmaf(aim, I2H, -aih) + aim * I2L;
    f32x4 pv = {are * 1.44269504088896340736f, aih, 2.0f * bf2f(Cre[d * NM_ + tid]) * dt, 2.0f * bf2f(Cim[d * NM_ + tid]) * dt};
    *(LAS f32x4*)(smem + SS_PAR + tid * 16) = pv;
    *(LAS float*)(smem + SS_PARL + tid * 4) = ail;
  }
  if (tid < 16) *(LAS bf16_t*)(smem + SS_UL + tid * 2) = 0;
  if (tid < 48) *(LAS bf16_t*)(smem + SS_UL + (16 + 2048 + tid) * 2) = 0;
#pragma unroll
  for (int k = 0; k < 8; ++k) *(LAS bf16_t*)(smem + SS_UL + (16 + tid + 256 * k) * 2) = ucol[k];
  __syncthreads();
  {
    const int i = lane & 15, kk = lane >> 4;
    const float py = (float)(8 * i);
    const float px0 = (float)prow_of(i), px1 = (float)prow_of(16 + i), px2 = (float)prow_of(32 + i);
    f32x4 acc0 = {0, 0, 0, 0}, acc1 = {0, 0, 0, 0}, acc2 = {0, 0, 0, 0};
#pragma unroll 2
    for (int t = 0; t < 8; ++t) {
      const int n = 32 * wid + 4 * t + kk;
      const f32x4 pv = *(const LAS f32x4*)(smem + SS_PAR + n * 16);
      const float ail = *(const LAS float*)(smem + SS_PARL + n * 4);
      float yr, yi, er, ei;
      cexp_p(pv[0], pv[1], ail, py, yr, yi);
      cexp_p(pv[0], pv[1], ail, px0, er, ei);
      acc0 = __builtin_amdgcn_mfma_f32_16x16x4f32(pv[2] * er - pv[3] * ei, yr, acc0, 0, 0, 0);
      acc0 = __builtin_amdgcn_mfma_f32_16x16x4f32(-(pv[2] * ei + pv[3] * er), yi, acc0, 0, 0, 0);
      cexp_p(pv[0], pv[1], ail, px1, er, ei);
      acc1 = __builtin_amdgcn_mfma_f32_16x16x4f32(pv[2] * er - pv[3] * ei, yr, acc1, 0, 0, 0);
      acc1 = __builtin_amdgcn_mfma_f32_16x16x4f32(-(pv[2] * ei + pv[3] * er), yi, acc1, 0, 0, 0);
      cexp_p(pv[0], pv[1], ail, px2, er, ei);
      acc2 = __builtin_amdgcn_mfma_f32_16x16x4f32(pv[2] * er - pv[3] * ei, yr, acc2, 0, 0, 0);
      acc2 = __builtin_amdgcn_mfma_f32_16x16x4f32(-(pv[2] * ei + pv[3] * er), yi, acc2, 0, 0, 0);
    }
    LAS char* red = smem + SS_V + wid * 3072;
    *(LAS f32x4*)(red + lane * 16) = acc0;
    *(LAS f32x4*)(red + 1024 + lane * 16) = acc1;
    *(LAS f32x4*)(red + 2048 + lane * 16) = acc2;
  }
  __syncthreads();
  if (tid < 192) {
    const int rt = tid >> 6;
    f32x4 s = *(const LAS f32x4*)(smem + SS_V + rt * 1024 + lane * 16);
#pragma unroll
    for (int w = 1; w < 4; ++w) s += *(const LAS f32x4*)(smem + SS_V + w * 3072 + rt * 1024 + lane * 16);
#pragma unroll
    for (int j = 0; j < 4; ++j)
      *(LAS float*)(smem + SS_KQ + ((16 * rt + 4 * (lane >> 4) + j) * 16 + (lane & 15)) * 4) = s[j];
  }
  __syncthreads();
  for (int ch = tid; ch < 288; ch += 256) {
    u32x4 oh, ol;
#pragma unroll
    for (int e = 0; e < 8; e += 2) {
      float v[2];
#pragma unroll
      for (int z = 0; z < 2; ++z) {
        const int idx = 2047 - (ch * 8 + e + z);
        v[z] = idx >= 0 ? *(const LAS float*)(smem + SS_KQ + kq_index(idx) * 4) : 0.f;
      }
      const float h0 = rbf(v[0]), h1 = rbf(v[1]);
      oh[e >> 1] = pack2(h0, h1);
      ol[e >> 1] = pack2(v[0] - h0, v[1] - h1);
    }
    *(LAS u32x4*)(smem + SS_KH + ch * 16) = oh;
    *(LAS u32x4*)(smem + SS_KL + ch * 16) = ol;
  }
  for (int ch = tid; ch < 260; ch += 256) {
    const u32x4 lo = *(const LAS u32x4*)(smem + SS_UL + ch * 16);
    const u32x4 hi = *(const LAS u32x4*)(smem + SS_UL + ch * 16 + 16);
    unsigned w[8] = {lo[0], lo[1], lo[2], lo[3], hi[0], hi[1], hi[2], hi[3]};
#pragma unroll
    for (int rho = 0; rho < 8; ++rho) {
      u32x4 o;
#pragma unroll
      for (int j = 0; j < 4; ++j) {
        const int b = (rho >> 1) + j;
        o[j] = (rho & 1) ? __builtin_amdgcn_alignbit(w[b + 1], w[b], 16) : w[b];
      }
      *(LAS u32x4*)(smem + SS_V + rho * SS_VSTR + ch * 16) = o;
    }
  }
  __syncthreads();
  f32x4 acc[8];
#pragma unroll
  for (int r = 0; r < 8; ++r) acc[r] = (f32x4){0, 0, 0, 0};
  {
    const int ij = lane & 15, g = lane >> 4;
    const int rho = (ij + 1) & 7, h = (ij + 1) >> 3;
    const LAS char* gbase = smem + SS_V + rho * SS_VSTR + (8 * g + 8 * h) * 2;
    const int kofs = (2032 - 16 * ij + 8 * g) * 2;
#pragma unroll 1
    for (int cc = 0; cc < 2; ++cc) {
      const int c = 2 * wid + cc;
      bf16x8 G[8];
#pragma unroll
      for (int b = 0; b < 8; ++b) G[b] = *(const LAS bf16x8*)(gbase + (256 * b + 32 * c) * 2);
#pragma unroll
      for (int a = 0; a < 8; ++a) {
        const bf16x8 fh = *(const LAS bf16x8*)(smem + SS_KH + kofs + (32 * c - 256 * a) * 2);
        const bf16x8 fl = *(const LAS bf16x8*)(smem + SS_KL + kofs + (32 * c - 256 * a) * 2);
#pragma unroll
        for (int b = 0; b + a < 8; ++b) {
          acc[a + b] = __builtin_amdgcn_mfma_f32_16x16x32_bf16(G[b], fh, acc[a + b], 0, 0, 0);
          acc[a + b] = __builtin_amdgcn_mfma_f32_16x16x32_bf16(G[b], fl, acc[a + b], 0, 0, 0);
        }
      }
    }
  }
  __syncthreads();
#pragma unroll
  for (int r = 0; r < 8; ++r) *(LAS f32x4*)(smem + SS_V + (wid * 8 + r) * 1024 + lane * 16) = acc[r];
  __syncthreads();
  const float dsk = bf2f(Dskip[d]);
#pragma unroll
  for (int rr = 0; rr < 2; ++rr) {
    const int r = 2 * wid + rr;
    f32x4 s = *(const LAS f32x4*)(smem + SS_V + r * 1024 + lane * 16);
#pragma unroll
    for (int w = 1; w < 4; ++w) s += *(const LAS f32x4*)(smem + SS_V + (w * 8 + r) * 1024 + lane * 16);
    const int l0 = 256 * r + 16 * (lane & 15) + 4 * (lane >> 4);
    const bf16x4 uu = *(const LAS bf16x4*)(smem + SS_UL + (16 + l0) * 2);
    float o[4];
#pragma unroll
    for (int j = 0; j < 4; ++j) o[j] = rbf(s[j]) + rbf(bf2f((bf16_t)uu[j]) * dsk);
    u32x2 ov = {pack2(o[0], o[1]), pack2(o[2], o[3])};
    *(u32x2*)(aT + (size_t)d * L_ + l0) = ov;
  }
}

__global__ __launch_bounds__(256) void k1_prep(const bf16_t* __restrict__ x, const bf16_t* __restrict__ logA, const bf16_t* __restrict__ Aim,
                                               const bf16_t* __restrict__ Cre, const bf16_t* __restrict__ Cim, const bf16_t* __restrict__ logdt,
                                               const bf16_t* __restrict__ Dskip, const bf16_t* __restrict__ gate_w,
                                               bf16_t* __restrict__ hT, bf16_t* __restrict__ aT, float* __restrict__ gate) {
  extern __shared__ __attribute__((aligned(16))) char smem_[];
  LAS char* smem = (LAS char*)smem_;
  if (blockIdx.x < LN_WGS) ln_block(x, gate_w, hT, gate, blockIdx.x, smem);
  else ssm_block(x, logA, Aim, Cre, Cim, logdt, Dskip, aT, blockIdx.x - LN_WGS, smem);
}
constexpr int K1_LDS = (SS_END > LN_ROWS * LN_STRIDE) ? SS_END : LN_ROWS * LN_STRIDE;

template <int AB> struct GemmCfg {
  static constexpr int ACT_BYTES = AB * 16 * 128;
  static constexpr int STAGE = ACT_BYTES + 64 * 256;
};

#define WAIT_VM(n) asm volatile("s_waitcnt vmcnt(%0)" ::"n"(n) : "memory")

template <int AB, int ABW, int NGA, int S, int NT, bool ACT_A, int LDA, int LDW>
__device__ __forceinline__ void gemm_wave(const bf16_t* __restrict__ actg,
                                          const bf16_t* __restrict__ wg,
                                          LAS char* smem, int wid, int lane, int ab0, f32x4 (&acc)[ABW][4]) {
  constexpr int STAGE = GemmCfg<AB>::STAGE, ACTB = GemmCfg<AB>::ACT_BYTES;
  constexpr int NG = NGA + 4;
  const int ww = wid & 1;
  const int aq0 = 3 - wid;
  const unsigned aoff = (unsigned)(((lane >> 3) + 8 * aq0) * LDA + (((lane & 7) ^ (lane >> 3)) * 8)) * 2u;
  const int wf = (lane >> 4) | (((wid >> 1) & 1) << 2);
  const unsigned woff = (unsigned)(((lane >> 4) + 4 * wid) * LDW + (((lane & 15) ^ (wf << 1)) * 8)) * 2u;
  const char* ap = (const char*)actg + aoff;
  const char* wp = (const char*)wg + woff;
  auto issue = [&](int t, int buf) {
    LAS char* sb = smem + buf * STAGE;
    const char* a = ap + (size_t)t * 128;
    const char* w = wp + (size_t)t * (64 * LDW * 2);
#pragma unroll
    for (int j = 0; j < NGA; ++j)
      __builtin_amdgcn_global_load_lds((const unsigned*)(a + (size_t)j * (32 * LDA * 2)), (LAS unsigned*)(sb + (aq0 + 4 * j) * 1024), 16, 0, 0);
#pragma unroll
    for (int j = 0; j < 4; ++j)
      __builtin_amdgcn_global_load_lds((const unsigned*)(w + (size_t)j * (16 * LDW * 2)), (LAS unsigned*)(sb + ACTB + (wid + 4 * j) * 1024), 16, 0, 0);
  };
  const int c = lane & 15, g = lane >> 4;
  const int ard0 = (16 * ab0 + c) * 128 + ((g ^ (c & 7)) * 16);
  const int ard1 = (16 * ab0 + c) * 128 + (((4 + g) ^ (c & 7)) * 16);
  const int q4 = (lane & 15) >> 2, p4 = lane & 3;
  const int ff = q4 | ((g & 1) << 2);
  int wrd[4];
#pragma unroll
  for (int nb = 0; nb < 4; ++nb) wrd[nb] = ACTB + (8 * g + q4) * 256 + (((4 * ww + nb) ^ ff) * 32) + p4 * 8;

  bf16x8 fa0[ABW], fw0[4], fa1[ABW], fw1[4];
  auto rd = [&](bf16x8 (&fa)[ABW], bf16x8 (&fw)[4], int buf, int kk) {
    const LAS char* sb = smem + buf * STAGE;
#pragma unroll
    for (int nb = 0; nb < 4; ++nb) {
      const bf16x4 v0 = __builtin_amdgcn_ds_read_tr16_b64_v4i16((LAS bf16x4*)(sb + wrd[nb] + kk * 8192));
      const bf16x4 v1 = __builtin_amdgcn_ds_read_tr16_b64_v4i16((LAS bf16x4*)(sb + wrd[nb] + kk * 8192 + 1024));
      fw[nb] = __builtin_shufflevector(v0, v1, 0, 1, 2, 3, 4, 5, 6, 7);
    }
#pragma unroll
    for (int ab = 0; ab < ABW; ++ab) fa[ab] = *(const LAS bf16x8*)(sb + (kk ? ard1 : ard0) + ab * 2048);
  };
  auto mm = [&](bf16x8 (&fa)[ABW], bf16x8 (&fw)[4]) {
#pragma unroll
    for (int ab = 0; ab < ABW; ++ab)
#pragma unroll
      for (int nb = 0; nb < 4; ++nb)
        acc[ab][nb] = ACT_A ? __builtin_amdgcn_mfma_f32_16x16x32_bf16(fa[ab], fw[nb], acc[ab][nb], 0, 0, 0)
                            : __builtin_amdgcn_mfma_f32_16x16x32_bf16(fw[nb], fa[ab], acc[ab][nb], 0, 0, 0);
  };
#define BARRIER() do { asm volatile("s_waitcnt lgkmcnt(0)" ::: "memory"); __builtin_amdgcn_s_barrier(); asm volatile("" ::: "memory"); } while (0)
#pragma unroll
  for (int s = 0; s < S; ++s) issue(s, s);
  WAIT_VM((S - 1) * NG);
  BARRIER();
  rd(fa0, fw0, 0, 0);
  int buf = 0;
#pragma unroll 1
  for (int t = 0; t < NT - S; ++t) {
    const int nbuf = (buf + 1 == S) ? 0 : buf + 1;
    rd(fa1, fw1, buf, 1);
    mm(fa0, fw0);
    WAIT_VM((S - 2) * NG);
    BARRIER();
    __builtin_amdgcn_sched_barrier(0);
    issue(t + S, buf);
    rd(fa0, fw0, nbuf, 0);
    mm(fa1, fw1);
    buf = nbuf;
  }
#pragma unroll
  for (int i = 0; i < S; ++i) {
    const int nbuf = (buf + 1 == S) ? 0 : buf + 1;
    rd(fa1, fw1, buf, 1);
    mm(fa0, fw0);
    if (i < S - 1) {
      if (i == 0) WAIT_VM((S >= 2 ? S - 2 : 0) * NG);
      else if (i == 1) WAIT_VM((S >= 3 ? S - 3 : 0) * NG);
      else if (i == 2) WAIT_VM((S >= 4 ? S - 4 : 0) * NG);
      else WAIT_VM(0);
      BARRIER();
      __builtin_amdgcn_sched_barrier(0);
      rd(fa0, fw0, nbuf, 0);
    }
    mm(fa1, fw1);
    buf = nbuf;
  }
}

__device__ __forceinline__ float gelu_tanh(float x) {
  const float y = 0.7978845608028654f * (x + 0.044715f * x * x * x);
  const float e = __expf(2.0f * y);
  const float t = 1.0f - 2.0f / (e + 1.0f);
  return 0.5f * x * (1.0f + t);
}

constexpr int G1_S = 4;
constexpr int G1_LDS = G1_S * GemmCfg<6>::STAGE;
__global__ __launch_bounds__(256) void k2_gemm1(const bf16_t* __restrict__ hT, const bf16_t* __restrict__ w1, const bf16_t* __restrict__ b1,
                                                bf16_t* __restrict__ u) {
  extern __shared__ __attribute__((aligned(16))) char smem_[];
  LAS char* smem = (LAS char*)smem_;
  const int wid = __builtin_amdgcn_readfirstlane(threadIdx.x >> 6), lane = threadIdx.x & 63;
  const int b = blockIdx.x;
  const int id = (b & 7) * 27 + (b >> 3);
  const int nt = id / 6, mt = id % 6;
  const int d0 = mt * 96, n0 = nt * 128;
  f32x4 acc[3][4];
#pragma unroll
  for (int a = 0; a < 3; ++a)
#pragma unroll
    for (int n = 0; n < 4; ++n) acc[a][n] = (f32x4){0, 0, 0, 0};
  const int wa = wid >> 1, ww = wid & 1;
  gemm_wave<6, 3, 3, G1_S, 32, false, L_, F_>(hT + (size_t)d0 * L_, w1 + n0, smem, wid, lane, 3 * wa, acc);
  const int fq = lane >> 4, c = lane & 15;
#pragma unroll
  for (int nb = 0; nb < 4; ++nb) {
    const int f = n0 + 64 * ww + 16 * nb + 4 * fq;
    const bf16x4 bv = *(const bf16x4*)(b1 + f);
#pragma unroll
    for (int ab = 0; ab < 3; ++ab) {
      const int d = d0 + 48 * wa + 16 * ab + c;
      float o[4];
#pragma unroll
      for (int j = 0; j < 4; ++j) o[j] = gelu_tanh(rbf(rbf(acc[ab][nb][j]) + bf2f((bf16_t)bv[j])));
      u32x2 ov = {pack2(o[0], o[1]), pack2(o[2], o[3])};
      *(u32x2*)(u + (size_t)d * F_ + f) = ov;
    }
  }
}

constexpr int G2_S = 4;
constexpr int G2_LDS = G2_S * GemmCfg<9>::STAGE;
template <int ABW, int NGA>
__device__ __forceinline__ void g2_body(const bf16_t* __restrict__ u, const bf16_t* __restrict__ w2, float* __restrict__ part,
                                        LAS char* smem, int wid, int lane, int d0, int s0, int ks, int ab0) {
  f32x4 acc[ABW][4];
#pragma unroll
  for (int a = 0; a < ABW; ++a)
#pragma unroll
    for (int n = 0; n < 4; ++n) acc[a][n] = (f32x4){0, 0, 0, 0};
  gemm_wave<9, ABW, NGA, G2_S, 18, true, F_, L_>(u + (size_t)d0 * F_ + ks * 1152, w2 + (size_t)ks * 1152 * L_ + s0, smem, wid, lane, ab0, acc);
  const int fq = lane >> 4, c = lane & 15, ww = wid & 1;
#pragma unroll
  for (int nb = 0; nb < 4; ++nb) {
    const int s = s0 + 64 * ww + 16 * nb + c;
#pragma unroll
    for (int ab = 0; ab < ABW; ++ab) {
      const int d = d0 + 16 * (ab0 + ab) + 4 * fq;
      *(f32x4*)(part + ((size_t)ks * L_ + s) * D_ + d) = acc[ab][nb];
    }
  }
}
__global__ __launch_bounds__(256) void k3_gemm2(const bf16_t* __restrict__ u, const bf16_t* __restrict__ w2, float* __restrict__ part) {
  extern __shared__ __attribute__((aligned(16))) char smem_[];
  LAS char* smem = (LAS char*)smem_;
  const int wid = __builtin_amdgcn_readfirstlane(threadIdx.x >> 6), lane = threadIdx.x & 63;
  const int b = blockIdx.x;
  const int id = (b & 7) * 32 + (b >> 3);
  const int st = id >> 4, ks = (id >> 2) & 3, dtile = id & 3;
  const int d0 = dtile * 144, s0 = st * 128;
  if (wid < 2) g2_body<5, 4>(u, w2, part, smem, wid, lane, d0, s0, ks, 0);
  else g2_body<4, 5>(u, w2, part, smem, wid, lane, d0, s0, ks, 5);
}

__global__ __launch_bounds__(256) void k4_combine(const float* __restrict__ part, const bf16_t* __restrict__ b2, const float* __restrict__ gate,
                                                  const bf16_t* __restrict__ aT, bf16_t* __restrict__ out) {
  const int idx = blockIdx.x * 256 + threadIdx.x;
  const int s = idx / 72, d0 = (idx % 72) * 8;
  const float g = gate[s], omg = rbf(1.0f - g), bb = bf2f(b2[s]);
  float o[8];
#pragma unroll
  for (int h = 0; h < 2; ++h) {
    f32x4 v = *(const f32x4*)(part + (size_t)s * D_ + d0 + 4 * h);
#pragma unroll
    for (int k = 1; k < 4; ++k) v += *(const f32x4*)(part + ((size_t)k * L_ + s) * D_ + d0 + 4 * h);
#pragma unroll
    for (int j = 0; j < 4; ++j) {
      const float bv = rbf(rbf(v[j]) + bb);
      const float a = bf2f(aT[(size_t)(d0 + 4 * h + j) * L_ + s]);
      o[4 * h + j] = rbf(g * a) + rbf(omg * bv);
    }
  }
  u32x4 ov = {pack2(o[0], o[1]), pack2(o[2], o[3]), pack2(o[4], o[5]), pack2(o[6], o[7])};
  *(u32x4*)(out + (size_t)s * D_ + d0) = ov;
}

extern "C" void kernel_launch(void* const* d_in, const int* in_sizes, int n_in, void* d_out, int out_size, void* d_ws, size_t ws_size,
                              hipStream_t stream) {
  const bf16_t* x = (const bf16_t*)d_in[0];
  const bf16_t* logA = (const bf16_t*)d_in[1];
  const bf16_t* Aim = (const bf16_t*)d_in[2];
  const bf16_t* Cre = (const bf16_t*)d_in[3];
  const bf16_t* Cim = (const bf16_t*)d_in[4];
  const bf16_t* logdt = (const bf16_t*)d_in[5];
  const bf16_t* Dskip = (const bf16_t*)d_in[6];
  const bf16_t* w1 = (const bf16_t*)d_in[8];
  const bf16_t* b1 = (const bf16_t*)d_in[9];
  const bf16_t* w2 = (const bf16_t*)d_in[10];
  const bf16_t* b2 = (const bf16_t*)d_in[11];
  const bf16_t* gate_w = (const bf16_t*)d_in[12];
  char* ws = (char*)d_ws;
  bf16_t* hT = (bf16_t*)(ws + WS_HT);
  bf16_t* aT = (bf16_t*)(ws + WS_AT);
  float* gate = (float*)(ws + WS_G);
  bf16_t* u = (bf16_t*)(ws + WS_U);
  float* part = (float*)(ws + WS_P);
  (void)hipFuncSetAttribute((const void*)k1_prep, hipFuncAttributeMaxDynamicSharedMemorySize, K1_LDS);
  (void)hipFuncSetAttribute((const void*)k2_gemm1, hipFuncAttributeMaxDynamicSharedMemorySize, G1_LDS);
  (void)hipFuncSetAttribute((const void*)k3_gemm2, hipFuncAttributeMaxDynamicSharedMemorySize, G2_LDS);
  k1_prep<<<LN_WGS + D_, 256, K1_LDS, stream>>>(x, logA, Aim, Cre, Cim, logdt, Dskip, gate_w, hT, aT, gate);
  k2_gemm1<<<216, 256, G1_LDS, stream>>>(hT, w1, b1, u);
  k3_gemm2<<<256, 256, G2_LDS, stream>>>(u, w2, part);
  k4_combine<<<L_ * 72 / 256, 256, 0, stream>>>(part, b2, gate, aT, (bf16_t*)d_out);
}
```

```cpp
#include <hip/hip_runtime.h>
#include <hip/hip_bf16.h>
#include <stdint.h>

typedef unsigned short bf16_t;
typedef short bf16x8 __attribute__((ext_vector_type(8)));
typedef short bf16x4 __attribute__((ext_vector_type(4)));
typedef float f32x4 __attribute__((ext_vector_type(4)));
typedef unsigned u32x4 __attribute__((ext_vector_type(4)));
typedef unsigned u32x2 __attribute__((ext_vector_type(2)));
#define LAS __attribute__((address_space(3)))

constexpr int L_ = 2048, D_ = 576, NM_ = 128, F_ = 4608;
constexpr int HT_LD = 2048, U_LD = 4608;

__device__ __forceinline__ float bf2f(bf16_t v) { return __uint_as_float(((unsigned)v) << 16); }
__device__ __forceinline__ bf16_t f2bf(float f) {
  __hip_bfloat16 h = __float2bfloat16(f);
  return __builtin_bit_cast(unsigned short, h);
}
__device__ __forceinline__ float rbf(float f) { return bf2f(f2bf(f)); }
__device__ __forceinline__ unsigned pack2(float lo, float hi) { unsigned r; asm("v_cvt_pk_bf16_f32 %0, %1, %2" : "=v"(r) : "v"(lo), "v"(hi)); return r; }
typedef float f32x2 __attribute__((ext_vector_type(2)));

#define STAMP_AT(i)
#define SSTAMP(i)
#define GSTAMP(i) STAMP_AT(i)
constexpr size_t WS_HT = 0;
constexpr size_t WS_AT = WS_HT + (size_t)D_ * HT_LD * 2;
constexpr size_t WS_G  = WS_AT + (size_t)D_ * L_ * 2;
constexpr size_t WS_U  = WS_G + (size_t)L_ * 4;
constexpr size_t WS_CNT = WS_U + (size_t)D_ * U_LD * 2;
constexpr size_t WS_P  = WS_CNT + 64 * 64 * 4;

constexpr int SS_PAR = 0;
constexpr int SS_PARL = SS_PAR + 2048;
constexpr int SS_KQ = SS_PARL + 512;
constexpr int SS_KH = SS_KQ + 3072;
constexpr int SS_KL = SS_KH + 4608;
constexpr int SS_UL = SS_KL + 4608;
constexpr int SS_V = SS_UL + 4224;
constexpr int SS_VSTR = 4160;
constexpr int SS_END = SS_V + 8 * SS_VSTR;
constexpr int SS_LN = 2 * SS_END;
constexpr int LN_STRIDE = 1152 + 16;
constexpr int SS_PF = SS_LN + 8 * LN_STRIDE;
constexpr int K1_LDS = SS_PF + 2048;

__device__ __forceinline__ int prow_of(int r) {
  if (r < 16) return 128 * (r >> 3) + (r & 7);
  if (r < 24) return 256 + 128 * ((r - 16) >> 2) + 2 * ((r - 16) & 3);
  if (r < 32) return 512 + 128 * ((r - 24) >> 1) + 4 * ((r - 24) & 1);
  if (r <= 40) return 1024 + 128 * (r - 32);
  return 0;
}
__device__ __forceinline__ int kq_index(int l) {
  const int pos = (int)rbf((float)l);
  int e = (31 - __builtin_clz(pos | 1)) - 7;
  e = e < 0 ? 0 : e;
  const int q = pos - (e ? (128 << e) : 0);
  const int row = (e ? 8 + 8 * e : 0) + (q >> 7) * (8 >> e) + ((q & 7) >> e);
  return row * 16 + ((q >> 3) & 15);
}
__device__ __forceinline__ void cexp_p(float are2, float aih, float ail, float p, float& er, float& ei) {
  const float mag = __builtin_amdgcn_exp2f(are2 * p);
  const float pr = aih * p;
  const float er2 = __builtin_fmaf(aih, p, -pr);
  const float rr = __builtin_amdgcn_fractf(pr) + __builtin_fmaf(ail, p, er2);
  er = mag * __builtin_amdgcn_cosf(rr);
  ei = mag * __builtin_amdgcn_sinf(rr);
}
__device__ __forceinline__ void cexp_p2(float are2, float aih, float ail, f32x2 p, f32x2& e0, f32x2& e1) {
  const f32x2 x = p * are2;
  const f32x2 pr = p * aih;
  const f32x2 er2 = __builtin_elementwise_fma(p, (f32x2){aih, aih}, -pr);
  const f32x2 lo = __builtin_elementwise_fma(p, (f32x2){ail, ail}, er2);
  f32x2 rr = {__builtin_amdgcn_fractf(pr[0]), __builtin_amdgcn_fractf(pr[1])};
  rr += lo;
  const float m0 = __builtin_amdgcn_exp2f(x[0]), m1 = __builtin_amdgcn_exp2f(x[1]);
  e0 = (f32x2){__builtin_amdgcn_cosf(rr[0]), __builtin_amdgcn_sinf(rr[0])} * m0;
  e1 = (f32x2){__builtin_amdgcn_cosf(rr[1]), __builtin_amdgcn_sinf(rr[1])} * m1;
}
__device__ __forceinline__ void split2(float a, float b, unsigned& hi, unsigned& lo) {
  hi = pack2(a, b);
  const f32x2 r = (f32x2){a, b} - (f32x2){__uint_as_float(hi << 16), __uint_as_float(hi & 0xffff0000u)};
  lo = pack2(r[0], r[1]);
}

__device__ __forceinline__ float wave_sum(float v) {
  v += __builtin_bit_cast(float, __builtin_amdgcn_update_dpp(0, __builtin_bit_cast(int, v), 0xB1, 0xF, 0xF, true));
  v += __builtin_bit_cast(float, __builtin_amdgcn_update_dpp(0, __builtin_bit_cast(int, v), 0x4E, 0xF, 0xF, true));
  v += __builtin_bit_cast(float, __builtin_amdgcn_update_dpp(0, __builtin_bit_cast(int, v), 0x141, 0xF, 0xF, true));
  v += __builtin_bit_cast(float, __builtin_amdgcn_update_dpp(0, __builtin_bit_cast(int, v), 0x140, 0xF, 0xF, true));
  const float r0 = __builtin_bit_cast(float, __builtin_amdgcn_readlane(__builtin_bit_cast(int, v), 0));
  const float r1 = __builtin_bit_cast(float, __builtin_amdgcn_readlane(__builtin_bit_cast(int, v), 16));
  const float r2 = __builtin_bit_cast(float, __builtin_amdgcn_readlane(__builtin_bit_cast(int, v), 32));
  const float r3 = __builtin_bit_cast(float, __builtin_amdgcn_readlane(__builtin_bit_cast(int, v), 48));
  return (r0 + r1) + (r2 + r3);
}

struct SsmArgs {
  const bf16_t* x; const bf16_t* logA; const bf16_t* Aim; const bf16_t* Cre; const bf16_t* Cim; const bf16_t* logdt; const bf16_t* Dskip;
  const bf16_t* gate_w; bf16_t* hT; bf16_t* aT; float* gate; const bf16_t* pf;
  unsigned* cnt;
};

template <int ROWBYTES, int ROWS>
__device__ __forceinline__ void prefetch_rows(const char* base, size_t pitch, LAS char* scratch, int tid) {
  constexpr int SEG = ROWBYTES / 64;
  constexpr int TOT = SEG * ROWS;
#pragma unroll 1
  for (int i = tid; i < TOT; i += 512) {
    const int r = i / SEG, sgm = i - r * SEG;
    __builtin_amdgcn_global_load_lds((const unsigned*)(base + (size_t)r * pitch + sgm * 64), (LAS unsigned*)(scratch + (tid >> 6) * 256), 4, 0, 0);
  }
}

#define LDS_BARRIER() do { asm volatile("" ::: "memory"); __builtin_amdgcn_s_waitcnt(0xC07F); __builtin_amdgcn_s_barrier(); asm volatile("" ::: "memory"); } while (0)
template <bool DO_LN>
__device__ __forceinline__ void ssm_wg(const SsmArgs& a, int d0, int lnrow0, const char* pfbase, size_t pfpitch, LAS char* smem_all) {
  const int tid = threadIdx.x, wid8 = __builtin_amdgcn_readfirstlane(tid >> 6), lane = tid & 63;
  const int half = wid8 >> 2, wid = wid8 & 3, t256 = tid & 255;
  const int d = d0 + half;
  LAS char* smem = smem_all + half * SS_END;
  SSTAMP(0);
  const int mi = d * NM_ + (t256 & 127);
  const bf16_t r_dt = a.logdt[d], r_ds = a.Dskip[d], r_la = a.logA[mi], r_ai = a.Aim[mi], r_cr = a.Cre[mi], r_ci = a.Cim[mi];
  const float dt = rbf(expf(bf2f(r_dt)));
  const float dsk = bf2f(r_ds);
  {
    const float are = -rbf(expf(bf2f(r_la))) * dt;
    const float aim = bf2f(r_ai) * dt;
    const float I2H = 0.159154936671257019043f;
    const float I2L = 6.42063824376987e-09f;
    const float aih = aim * I2H;
    const float ail = __builtin_fmaf(aim, I2H, -aih) + aim * I2L;
    f32x4 pv = {are * 1.44269504088896340736f, aih, 2.0f * bf2f(r_cr) * dt, 2.0f * bf2f(r_ci) * dt};
    *(LAS f32x4*)(smem + SS_PAR + (t256 & 127) * 16) = pv;
    *(LAS float*)(smem + SS_PARL + (t256 & 127) * 4) = ail;
  }
  LDS_BARRIER();
  SSTAMP(1);
  u32x2 lnv[3], lnw[3];
  if (DO_LN) {
    const bf16_t* xr = a.x + (size_t)(lnrow0 + wid8) * D_;
#pragma unroll
    for (int k = 0; k < 3; ++k) {
      const int ch = (k < 2 || lane < 16) ? lane + 64 * k : 0;
      lnv[k] = *(const u32x2*)(xr + ch * 4);
      lnw[k] = *(const u32x2*)(a.gate_w + ch * 4);
    }
  }
  unsigned ucol[4];
#pragma unroll
  for (int k = 0; k < 4; ++k) ucol[k] = *(const unsigned*)(a.x + (size_t)(tid + 512 * k) * D_ + d0);
  __builtin_amdgcn_sched_barrier(0);
  {
    const int i = lane & 15, g = lane >> 4;
    const float py = (float)(8 * i);
    const float px[3] = {(float)prow_of(i), (float)prow_of(16 + i), (float)prow_of(32 + i)};
    f32x4 acc0 = {0, 0, 0, 0}, acc1 = {0, 0, 0, 0}, acc2 = {0, 0, 0, 0};
#pragma unroll 1
    for (int t = 0; t < 2; ++t) {
      u32x4 yh, yl, xh[3], xl[3];
#pragma unroll
      for (int m = 0; m < 4; ++m) {
        const int n = 32 * wid + 16 * t + 4 * g + m;
        const f32x4 pv = *(const LAS f32x4*)(smem + SS_PAR + n * 16);
        const float ail = *(const LAS float*)(smem + SS_PARL + n * 4);
        f32x2 ey, e0, e1, e2;
        cexp_p2(pv[0], pv[1], ail, (f32x2){py, px[0]}, ey, e0);
        cexp_p2(pv[0], pv[1], ail, (f32x2){px[1], px[2]}, e1, e2);
        const float yr = pv[2] * ey[0] - pv[3] * ey[1], yi = pv[2] * ey[1] + pv[3] * ey[0];
        { unsigned h_, l_; split2(yr, -yi, h_, l_); yh[m] = h_; yl[m] = l_; }
        { unsigned h_, l_; split2(e0[0], e0[1], h_, l_); xh[0][m] = h_; xl[0][m] = l_; }
        { unsigned h_, l_; split2(e1[0], e1[1], h_, l_); xh[1][m] = h_; xl[1][m] = l_; }
        { unsigned h_, l_; split2(e2[0], e2[1], h_, l_); xh[2][m] = h_; xl[2][m] = l_; }
      }
      const bf16x8 YH = __builtin_bit_cast(bf16x8, yh), YL = __builtin_bit_cast(bf16x8, yl);
      acc0 = __builtin_amdgcn_mfma_f32_16x16x32_bf16(__builtin_bit_cast(bf16x8, xl[0]), YH, acc0, 0, 0, 0);
      acc1 = __builtin_amdgcn_mfma_f32_16x16x32_bf16(__builtin_bit_cast(bf16x8, xl[1]), YH, acc1, 0, 0, 0);
      acc2 = __builtin_amdgcn_mfma_f32_16x16x32_bf16(__builtin_bit_cast(bf16x8, xl[2]), YH, acc2, 0, 0, 0);
      acc0 = __builtin_amdgcn_mfma_f32_16x16x32_bf16(__builtin_bit_cast(bf16x8, xh[0]), YL, acc0, 0, 0, 0);
      acc1 = __builtin_amdgcn_mfma_f32_16x16x32_bf16(__builtin_bit_cast(bf16x8, xh[1]), YL, acc1, 0, 0, 0);
      acc2 = __builtin_amdgcn_mfma_f32_16x16x32_bf16(__builtin_bit_cast(bf16x8, xh[2]), YL, acc2, 0, 0, 0);
      acc0 = __builtin_amdgcn_mfma_f32_16x16x32_bf16(__builtin_bit_cast(bf16x8, xh[0]), YH, acc0, 0, 0, 0);
      acc1 = __builtin_amdgcn_mfma_f32_16x16x32_bf16(__builtin_bit_cast(bf16x8, xh[1]), YH, acc1, 0, 0, 0);
      acc2 = __builtin_amdgcn_mfma_f32_16x16x32_bf16(__builtin_bit_cast(bf16x8, xh[2]), YH, acc2, 0, 0, 0);
    }
    LAS char* red = smem + SS_V + wid * 3072;
    *(LAS f32x4*)(red + lane * 16) = acc0;
    *(LAS f32x4*)(red + 1024 + lane * 16) = acc1;
    *(LAS f32x4*)(red + 2048 + lane * 16) = acc2;
  }
  SSTAMP(2);
  if (t256 < 16) *(LAS bf16_t*)(smem + SS_UL + t256 * 2) = 0;
  if (t256 < 48) *(LAS bf16_t*)(smem + SS_UL + (16 + 2048 + t256) * 2) = 0;
#pragma unroll
  for (int k = 0; k < 4; ++k) {
    *(LAS bf16_t*)(smem_all + SS_UL + (16 + tid + 512 * k) * 2) = (bf16_t)(ucol[k] & 0xffffu);
    *(LAS bf16_t*)(smem_all + SS_END + SS_UL + (16 + tid + 512 * k) * 2) = (bf16_t)(ucol[k] >> 16);
  }
  SSTAMP(3);
  if (DO_LN) {
    float xv[12], sum = 0.f, gs = 0.f;
#pragma unroll
    for (int k = 0; k < 3; ++k) {
      const bool ok = (k < 2 || lane < 16);
#pragma unroll
      for (int j = 0; j < 2; ++j) {
        const float a0 = ok ? __uint_as_float(lnv[k][j] << 16) : 0.f, a1 = ok ? __uint_as_float(lnv[k][j] & 0xffff0000u) : 0.f;
        const float w0 = __uint_as_float(lnw[k][j] << 16), w1 = __uint_as_float(lnw[k][j] & 0xffff0000u);
        xv[4 * k + 2 * j] = a0; xv[4 * k + 2 * j + 1] = a1;
        sum += a0 + a1;
        gs += rbf(a0 * w0) + rbf(a1 * w1);
      }
    }
    sum = wave_sum(sum); gs = wave_sum(gs);
    const float mean = sum * (1.0f / D_);
    float vs = 0.f;
#pragma unroll
    for (int k = 0; k < 3; ++k)
#pragma unroll
      for (int j = 0; j < 4; ++j) { const float dd = xv[4 * k + j] - mean; vs += (k < 2 || lane < 16) ? dd * dd : 0.f; }
    vs = wave_sum(vs);
    const float mb = rbf(mean);
    const float vb = rbf(vs * (1.0f / D_));
    const float den = rbf(sqrtf(rbf(vb + 1e-5f)));
    if (lane == 0) a.gate[lnrow0 + wid8] = rbf(1.0f / (1.0f + expf(-rbf(gs))));
#pragma unroll
    for (int k = 0; k < 3; ++k)
      if (k < 2 || lane < 16) {
        u32x2 o = {pack2(rbf(xv[4 * k] - mb) / den, rbf(xv[4 * k + 1] - mb) / den), pack2(rbf(xv[4 * k + 2] - mb) / den, rbf(xv[4 * k + 3] - mb) / den)};
        *(LAS u32x2*)(smem_all + SS_LN + wid8 * LN_STRIDE + (lane + 64 * k) * 8) = o;
      }
  }
  SSTAMP(4);
  if (DO_LN) prefetch_rows<1152, 64>(pfbase, pfpitch, smem_all + SS_PF, tid);
  else prefetch_rows<512, 1152>(pfbase, pfpitch, smem_all + SS_PF, tid);
  LDS_BARRIER();
  SSTAMP(5);
  if (DO_LN) {
#pragma unroll
    for (int k = 0; k < 2; ++k) {
      const int dd = tid + 512 * k;
      if (dd < D_) {
        unsigned v[8];
#pragma unroll
        for (int r = 0; r < 8; ++r) v[r] = *(const LAS bf16_t*)(smem_all + SS_LN + r * LN_STRIDE + dd * 2);
        u32x4 o = {v[0] | (v[1] << 16), v[2] | (v[3] << 16), v[4] | (v[5] << 16), v[6] | (v[7] << 16)};
        *(u32x4*)(a.hT + (size_t)dd * HT_LD + lnrow0) = o;
      }
    }
  }
  if (t256 < 192) {
    const int rt = wid;
    f32x4 s = *(const LAS f32x4*)(smem + SS_V + rt * 1024 + lane * 16);
#pragma unroll
    for (int w = 1; w < 4; ++w) s += *(const LAS f32x4*)(smem + SS_V + w * 3072 + rt * 1024 + lane * 16);
#pragma unroll
    for (int j = 0; j < 4; ++j)
      *(LAS float*)(smem + SS_KQ + ((16 * rt + 4 * (lane >> 4) + j) * 16 + (lane & 15)) * 4) = s[j];
  }
  LDS_BARRIER();
  for (int ch = t256; ch < 288; ch += 256) {
    u32x4 oh, ol;
#pragma unroll
    for (int e = 0; e < 8; e += 2) {
      float v[2];
#pragma unroll
      for (int z = 0; z < 2; ++z) {
        const int idx = 2047 - (ch * 8 + e + z);
        const float kv = *(const LAS float*)(smem + SS_KQ + kq_index(idx < 0 ? 0 : idx) * 4);
        v[z] = idx >= 0 ? kv : 0.f;
      }
      unsigned h_, l_; split2(v[0], v[1], h_, l_);
      oh[e >> 1] = h_; ol[e >> 1] = l_;
    }
    *(LAS u32x4*)(smem + SS_KH + ch * 16) = oh;
    *(LAS u32x4*)(smem + SS_KL + ch * 16) = ol;
  }
  for (int ch = t256; ch < 260; ch += 256) {
    const u32x4 lo = *(const LAS u32x4*)(smem + SS_UL + ch * 16);
    const u32x4 hi = *(const LAS u32x4*)(smem + SS_UL + ch * 16 + 16);
    unsigned w[8] = {lo[0], lo[1], lo[2], lo[3], hi[0], hi[1], hi[2], hi[3]};
#pragma unroll
    for (int rho = 0; rho < 8; ++rho) {
      u32x4 o;
#pragma unroll
      for (int j = 0; j < 4; ++j) {
        const int b = (rho >> 1) + j;
        o[j] = (rho & 1) ? __builtin_amdgcn_alignbit(w[b + 1], w[b], 16) : w[b];
      }
      *(LAS u32x4*)(smem + SS_V + rho * SS_VSTR + ch * 16) = o;
    }
  }
  LDS_BARRIER();
  SSTAMP(6);
  f32x4 acc[8];
#pragma unroll
  for (int r = 0; r < 8; ++r) acc[r] = (f32x4){0, 0, 0, 0};
  {
    const int ij = lane & 15, g = lane >> 4;
    const int rho = (ij + 1) & 7, h = (ij + 1) >> 3;
    const LAS char* gbase = smem + SS_V + rho * SS_VSTR + (8 * g + 8 * h) * 2;
    const int kofs = (2032 - 16 * ij + 8 * g) * 2;
#pragma unroll 1
    for (int cc = 0; cc < 2; ++cc) {
      const int c = 2 * wid + cc;
      bf16x8 G[8];
#pragma unroll
      for (int b = 0; b < 8; ++b) G[b] = *(const LAS bf16x8*)(gbase + (256 * b + 32 * c) * 2);
#pragma unroll
      for (int aa = 0; aa < 8; ++aa) {
        const bf16x8 fh = *(const LAS bf16x8*)(smem + SS_KH + kofs + (32 * c - 256 * aa) * 2);
        const bf16x8 fl = *(const LAS bf16x8*)(smem + SS_KL + kofs + (32 * c - 256 * aa) * 2);
#pragma unroll
        for (int b = 0; b + aa < 8; ++b) {
          acc[aa + b] = __builtin_amdgcn_mfma_f32_16x16x32_bf16(G[b], fh, acc[aa + b], 0, 0, 0);
          acc[aa + b] = __builtin_amdgcn_mfma_f32_16x16x32_bf16(G[b], fl, acc[aa + b], 0, 0, 0);
        }
      }
    }
  }
  SSTAMP(7);
  LDS_BARRIER();
#pragma unroll
  for (int r = 0; r < 8; ++r) *(LAS f32x4*)(smem + SS_V + (wid * 8 + r) * 1024 + lane * 16) = acc[r];
  LDS_BARRIER();
#pragma unroll
  for (int rr = 0; rr < 2; ++rr) {
    const int r = 2 * wid + rr;
    f32x4 s = *(const LAS f32x4*)(smem + SS_V + r * 1024 + lane * 16);
#pragma unroll
    for (int w = 1; w < 4; ++w) s += *(const LAS f32x4*)(smem + SS_V + (w * 8 + r) * 1024 + lane * 16);
    const int l0 = 256 * r + 16 * (lane & 15) + 4 * (lane >> 4);
    const bf16x4 uu = *(const LAS bf16x4*)(smem + SS_UL + (16 + l0) * 2);
    float o[4];
#pragma unroll
    for (int j = 0; j < 4; ++j) o[j] = rbf(s[j]) + rbf(bf2f((bf16_t)uu[j]) * dsk);
    u32x2 ov = {pack2(o[0], o[1]), pack2(o[2], o[3])};
    *(u32x2*)(a.aT + (size_t)d * L_ + l0) = ov;
  }
  asm volatile("s_waitcnt vmcnt(0)" ::: "memory");

}

__global__ __launch_bounds__(512) void k1_prep(SsmArgs a) {
  extern __shared__ __attribute__((aligned(16))) char smem_[];
  const int b = blockIdx.x;
  const int rb = (b & 7) * 32 + (b >> 3);
  if (b < 64 && threadIdx.x == 0) a.cnt[b * 64] = 0u;
  const char* pf = (const char*)a.pf + (size_t)(b >> 3) * 64 * (F_ * 2) + (b & 7) * 1152;
  ssm_wg<true>(a, 2 * b, 8 * rb, pf, (size_t)F_ * 2, (LAS char*)smem_);
}

template <int AB> struct GemmCfg {
  static constexpr int ACT_BYTES = AB * 16 * 128;
  static constexpr int STAGE = ACT_BYTES + 64 * 256;
};

#define WAIT_VM(n) asm volatile("s_waitcnt vmcnt(%0)" ::"n"(n) : "memory")

template <int N> __device__ __forceinline__ void wait_vm() { asm volatile("s_waitcnt vmcnt(%0)" ::"n"(N) : "memory"); }

template <int AB, int ABW, int NG, int SW, int SA, int NT, bool ACT_A, int LDA, int LDW, int GRP>
__device__ __forceinline__ void gemm_wave(const bf16_t* __restrict__ actg,
                                          const bf16_t* __restrict__ wg,
                                          LAS char* smem, int w4, int lane, int ab0, f32x4 (&acc)[ABW][4]) {
  constexpr int ACTB = AB * 2048, WB = 16384, ABASE = SW * WB;
  constexpr int S = GRP == 0 ? SW : SA;
  constexpr int SMAX = SW > SA ? SW : SA;
  const int ww = w4 & 1;
  const char* gp;
  if (GRP == 0) {
    const int wf = (lane >> 4) | (((w4 >> 1) & 1) << 2);
    gp = (const char*)wg + (unsigned)(((lane >> 4) + 4 * w4) * LDW + (((lane & 15) ^ (wf << 1)) * 8)) * 2u;
  } else {
    gp = (const char*)actg + (unsigned)(((lane >> 3) + 8 * w4) * LDA + (((lane & 7) ^ (lane >> 3)) * 8)) * 2u;
  }
  auto issue = [&](int t, int buf) {
    LAS char* sb = smem + (GRP == 0 ? buf * WB : ABASE + buf * ACTB) + w4 * 1024;
    const char* g = gp + (size_t)t * (GRP == 0 ? (size_t)64 * LDW * 2 : (size_t)128);
#pragma unroll
    for (int j = 0; j < NG; ++j)
      __builtin_amdgcn_global_load_lds((const unsigned*)(g + (size_t)j * (GRP == 0 ? 16 * LDW * 2 : 32 * LDA * 2)), (LAS unsigned*)(sb + j * 4096), 16, 0, 0);
  };
  const int c = lane & 15, g = lane >> 4;
  const int ard = ABASE + (16 * ab0 + c) * 128 + (((4 * GRP + g) ^ (c & 7)) * 16);
  const int q4 = (lane & 15) >> 2, p4 = lane & 3;
  const int ff = q4 | ((g & 1) << 2);
  int wrd[4];
#pragma unroll
  for (int nb = 0; nb < 4; ++nb) wrd[nb] = GRP * 8192 + (8 * g + q4) * 256 + (((4 * ww + nb) ^ ff) * 32) + p4 * 8;

  bf16x8 fa0[ABW], fa1[ABW];
  bf16x4 fw0[8], fw1[8];
  auto step = [&](bf16x8 (&faM)[ABW], bf16x4 (&fwM)[8], bf16x8 (&faR)[ABW], bf16x4 (&fwR)[8], int bw, int ba, bool doRead, int tI, int bI, bool doIssue) {
    const LAS char* sw = smem + bw * WB;
    const LAS char* sa = smem + ba * ACTB;
    LAS char* sb = smem + (GRP == 0 ? bI * WB : ABASE + bI * ACTB) + w4 * 1024;
    const char* gsrc = gp + (size_t)tI * (GRP == 0 ? (size_t)64 * LDW * 2 : (size_t)128);
    constexpr int NR = 8 + ABW, NM = 4 * ABW;
#pragma unroll
    for (int z = 0; z < NM; ++z) {
      const int r0 = (NM >= NR + 4) ? z : (3 * z + 1) / 2, r1 = (NM >= NR + 4) ? z + 1 : (3 * (z + 1) + 1) / 2;
      if (doRead) {
#pragma unroll
        for (int r = r0; r < r1; ++r) {
          if (r < 8) fwR[r] = __builtin_amdgcn_ds_read_tr16_b64_v4i16((LAS bf16x4*)(sw + wrd[r >> 1] + (r & 1) * 1024));
          else if (r < NR) faR[r - 8] = *(const LAS bf16x8*)(sa + ard + (r - 8) * 2048);
        }
      }
      {
        const int ab = z >> 2, nb = z & 3;
        const bf16x8 wv = __builtin_shufflevector(fwM[2 * nb], fwM[2 * nb + 1], 0, 1, 2, 3, 4, 5, 6, 7);
        acc[ab][nb] = ACT_A ? __builtin_amdgcn_mfma_f32_16x16x32_bf16(faM[ab], wv, acc[ab][nb], 0, 0, 0)
                            : __builtin_amdgcn_mfma_f32_16x16x32_bf16(wv, faM[ab], acc[ab][nb], 0, 0, 0);
      }
      if (doIssue && z >= NM - NG - 1 && z < NM - 1) {
        const int j = z - (NM - NG - 1);
        __builtin_amdgcn_global_load_lds((const unsigned*)(gsrc + (size_t)j * (GRP == 0 ? 16 * LDW * 2 : 32 * LDA * 2)), (LAS unsigned*)(sb + j * 4096), 16, 0, 0);
      }
      __builtin_amdgcn_sched_barrier(0);
    }
  };
#define BARRIER() do { asm volatile("" ::: "memory"); __builtin_amdgcn_s_waitcnt(0xC07F); __builtin_amdgcn_s_barrier(); asm volatile("" ::: "memory"); } while (0)
#define SCHED() __builtin_amdgcn_sched_barrier(0)
#pragma unroll
  for (int z = 0; z < 8; ++z) fw1[z] = (bf16x4){0, 0, 0, 0};
#pragma unroll
  for (int ab = 0; ab < ABW; ++ab) fa1[ab] = (bf16x8){0, 0, 0, 0, 0, 0, 0, 0};
#pragma unroll
  for (int s = 0; s < S - 1; ++s) issue(s, s);
  int bw = 0, ba = 0, ibuf = S - 1;
  auto adv = [&]() {
    ibuf = (ibuf + 1 == S) ? 0 : ibuf + 1;
    bw = (bw + 1 == SW) ? 0 : bw + 1;
    ba = (ba + 1 == SA) ? 0 : ba + 1;
  };
  constexpr int NMAIN = ((NT - (SMAX - 1)) / 2) * 2;
#pragma unroll 1
  for (int t = 0; t < NMAIN; t += 2) {
    wait_vm<(S - 2) * NG>();
    BARRIER();
    SCHED();
    if (!ACT_A && t == 0) GSTAMP(1);
    step(fa1, fw1, fa0, fw0, bw, ba, true, t + S - 1, ibuf, true);
    adv();
    wait_vm<(S - 2) * NG>();
    BARRIER();
    SCHED();
    step(fa0, fw0, fa1, fw1, bw, ba, true, t + S, ibuf, true);
    adv();
  }
#pragma unroll
  for (int t = NMAIN; t < NT; ++t) {
    switch ((t + S - 2 < NT - 1 ? S - 2 : NT - 1 - t)) {
      case 0: wait_vm<0>(); break;
      case 1: wait_vm<1 * NG>(); break;
      case 2: wait_vm<2 * NG>(); break;
      default: wait_vm<3 * NG>(); break;
    }
    BARRIER();
    SCHED();
    if ((t & 1) == 0) step(fa1, fw1, fa0, fw0, bw, ba, true, t + S - 1, ibuf, t + S - 1 < NT);
    else step(fa0, fw0, fa1, fw1, bw, ba, true, t + S - 1, ibuf, t + S - 1 < NT);
    adv();
  }
  __builtin_amdgcn_s_waitcnt(0xC07F);
  if ((NT & 1) == 0) step(fa1, fw1, fa0, fw0, 0, 0, false, 0, 0, false); else step(fa0, fw0, fa1, fw1, 0, 0, false, 0, 0, false);
}

__device__ __forceinline__ float gelu_tanh(float x) {
  const float c1 = -2.0f * 0.7978845608028654f * 1.4426950408889634f;
  const float c2 = c1 * 0.044715f;
  const float e = __builtin_amdgcn_exp2f(x * __builtin_fmaf(c2, x * x, c1));
  return x * __builtin_amdgcn_rcpf(1.0f + e);
}

template <int ABW, int GRP, int RSTR>
__device__ __forceinline__ void pair_reduce(f32x4 (&acc)[ABW][4], LAS char* smem, int w4, int lane) {
  __syncthreads();
  LAS char* mine = smem + (GRP * 4 + w4) * RSTR + lane * 16;
  const LAS char* theirs = smem + ((1 - GRP) * 4 + w4) * RSTR + lane * 16;
#pragma unroll
  for (int ab = 0; ab < ABW; ++ab)
#pragma unroll
    for (int h = 0; h < 2; ++h) *(LAS f32x4*)(mine + (ab * 2 + h) * 1024) = acc[ab][(GRP ? 0 : 2) + h];
  __syncthreads();
#pragma unroll
  for (int ab = 0; ab < ABW; ++ab)
#pragma unroll
    for (int h = 0; h < 2; ++h) acc[ab][(GRP ? 2 : 0) + h] += *(const LAS f32x4*)(theirs + (ab * 2 + h) * 1024);
}

#define G1SW 4
#define G1SA 4
constexpr int G1_LDS = G1SW * 16384 + G1SA * 6 * 2048;
template <int GRP>
__device__ __forceinline__ void g1_body(const bf16_t* __restrict__ hT, const bf16_t* __restrict__ w1, const bf16_t* __restrict__ b1,
                                        bf16_t* __restrict__ u, LAS char* smem, int w4, int lane, int d0, int n0) {
  f32x4 acc[3][4];
#pragma unroll
  for (int a = 0; a < 3; ++a)
#pragma unroll
    for (int n = 0; n < 4; ++n) acc[a][n] = (f32x4){0, 0, 0, 0};
  const int wa = w4 >> 1, ww = w4 & 1;
  gemm_wave<6, 3, GRP == 0 ? 4 : 3, G1SW, G1SA, 32, false, HT_LD, F_, GRP>(hT + (size_t)d0 * HT_LD, w1 + n0, smem, w4, lane, 3 * wa, acc);
  GSTAMP(2);
  pair_reduce<3, GRP, 6144>(acc, smem, w4, lane);
  const int fq = lane >> 4, c = lane & 15;
#pragma unroll
  for (int h = 0; h < 2; ++h) {
    const int nb = 2 * GRP + h;
    const int f = n0 + 64 * ww + 16 * nb + 4 * fq;
    const bf16x4 bv = *(const bf16x4*)(b1 + f);
#pragma unroll
    for (int ab = 0; ab < 3; ++ab) {
      const int d = d0 + 48 * wa + 16 * ab + c;
      float o[4];
#pragma unroll
      for (int j = 0; j < 4; ++j) o[j] = gelu_tanh(rbf(rbf(acc[ab][nb][j]) + bf2f((bf16_t)bv[j])));
      u32x2 ov = {pack2(o[0], o[1]), pack2(o[2], o[3])};
      *(u32x2*)(u + (size_t)d * U_LD + f) = ov;
    }
  }
}
constexpr int K2_LDS = G1_LDS > K1_LDS ? G1_LDS : K1_LDS;
__global__ __launch_bounds__(512) void k2_gemm1(const bf16_t* __restrict__ hT, const bf16_t* __restrict__ w1, const bf16_t* __restrict__ b1,
                                                bf16_t* __restrict__ u, SsmArgs a) {
  extern __shared__ __attribute__((aligned(16))) char smem_[];
  LAS char* smem = (LAS char*)smem_;
  const int wid = __builtin_amdgcn_readfirstlane(threadIdx.x >> 6), lane = threadIdx.x & 63;
  const int b = blockIdx.x;
  if (b >= 216) {
    const int j = b - 216;
    const char* pf = (const char*)a.pf + (size_t)(j >> 3) * 1152 * (L_ * 2) + (j & 7) * 512;
    ssm_wg<false>(a, 512 + 2 * j, 0, pf, (size_t)L_ * 2, smem);
    return;
  }
  const int id = (b & 7) * 27 + (b >> 3);
  const int nt = id / 6, mt = id % 6;
  const int d0 = mt * 96, n0 = nt * 128;
  GSTAMP(0);
  if (wid < 4) g1_body<0>(hT, w1, b1, u, smem, wid, lane, d0, n0);
  else g1_body<1>(hT, w1, b1, u, smem, wid - 4, lane, d0, n0);
  GSTAMP(3);
}

#define G2SW 4
#define G2SA 4
constexpr int G2_LDS = G2SW * 16384 + G2SA * 9 * 2048;
constexpr size_t SLAB_BYTES = (size_t)64 * 4 * 4 * 8 * 3 * 1024;
struct G2Args {
  const bf16_t* u; const bf16_t* w2; float* slab; unsigned* cnt; const bf16_t* b2; const float* gate; const bf16_t* aT; bf16_t* out;
};
template <int ABW, int NG, int GRP>
__device__ __forceinline__ void g2_body(const G2Args& a, LAS char* smem, int w4, int lane, int d0, int s0, int ks, int ab0, int tile) {
  f32x4 acc[ABW][4];
#pragma unroll
  for (int x = 0; x < ABW; ++x)
#pragma unroll
    for (int n = 0; n < 4; ++n) acc[x][n] = (f32x4){0, 0, 0, 0};
  gemm_wave<9, ABW, NG, G2SW, G2SA, 18, true, U_LD, L_, GRP>(a.u + (size_t)d0 * U_LD + ks * 1152, a.w2 + (size_t)ks * 1152 * L_ + s0, smem, w4, lane, ab0, acc);
  pair_reduce<ABW, GRP, 10240>(acc, smem, w4, lane);
  const int fq = lane >> 4, c = lane & 15, ww = w4 & 1, wid8 = GRP * 4 + w4;
  const __amdgpu_buffer_rsrc_t rs = __builtin_amdgcn_make_buffer_rsrc((void*)a.slab, 0, (int)SLAB_BYTES, 0x00020000);
  constexpr int NE = 2 * ABW;
#pragma unroll
  for (int e = 0; e < NE; ++e) {
    const int owner = e & 3;
    if (owner != ks) {
      const int off = ((((tile * 4 + owner) * 4 + ks) * 8 + wid8) * 3 + (e >> 2)) * 1024 + lane * 16;
      __builtin_amdgcn_raw_buffer_store_b128(__builtin_bit_cast(u32x4, acc[e >> 1][2 * GRP + (e & 1)]), rs, off, 0, 16);
    }
  }
  float gv[3], bv2[3], av[3][4];
#pragma unroll
  for (int sl = 0; sl < 3; ++sl) {
    const int e = ks + 4 * sl;
    const int ee = e < NE ? e : ks;
    const int s = s0 + 64 * ww + 16 * (2 * GRP + (ee & 1)) + c;
    const int d = d0 + 16 * (ab0 + (ee >> 1)) + 4 * fq;
    gv[sl] = a.gate[s];
    bv2[sl] = bf2f(a.b2[s]);
#pragma unroll
    for (int j = 0; j < 4; ++j) av[sl][j] = bf2f(a.aT[(size_t)(d + j) * L_ + s]);
  }
  asm volatile("s_waitcnt vmcnt(0)" ::: "memory");
  __syncthreads();
  if (wid8 == 0) {
    if (lane == 0) __hip_atomic_fetch_add(a.cnt + tile * 64, 1u, __ATOMIC_RELAXED, __HIP_MEMORY_SCOPE_AGENT);
    unsigned spins = 0;
    while ((unsigned)__builtin_amdgcn_readfirstlane(__hip_atomic_load(a.cnt + tile * 64, __ATOMIC_RELAXED, __HIP_MEMORY_SCOPE_AGENT)) < 4u) {
      __builtin_amdgcn_s_sleep(1);
      if (++spins > (1u << 24)) break;
    }
  }
  asm volatile("" ::: "memory");
  __syncthreads();
  asm volatile("" ::: "memory");
#pragma unroll
  for (int sl = 0; sl < 3; ++sl) {
    const int e0 = 4 * sl;
    if (e0 < NE) {
      if (ks + e0 < NE) {
        f32x4 v = {0, 0, 0, 0};
#pragma unroll
        for (int k = 0; k < 4; ++k) if (k == ks && e0 + k < NE) v = acc[(e0 + k) >> 1][2 * GRP + ((e0 + k) & 1)];
#pragma unroll
        for (int k = 1; k < 4; ++k) {
          const int src = (ks + k) & 3;
          const int off = ((((tile * 4 + ks) * 4 + src) * 8 + wid8) * 3 + sl) * 1024 + lane * 16;
          v += __builtin_bit_cast(f32x4, __builtin_amdgcn_raw_buffer_load_b128(rs, off, 0, 16));
        }
        const int e = ks + e0;
        const int s = s0 + 64 * ww + 16 * (2 * GRP + (e & 1)) + c;
        const int d = d0 + 16 * (ab0 + (e >> 1)) + 4 * fq;
        const float g = gv[sl], omg = rbf(1.0f - g);
        float o[4];
#pragma unroll
        for (int j = 0; j < 4; ++j) o[j] = rbf(g * av[sl][j]) + rbf(omg * rbf(rbf(v[j]) + bv2[sl]));
        u32x2 ov = {pack2(o[0], o[1]), pack2(o[2], o[3])};
        *(u32x2*)(a.out + (size_t)s * D_ + d) = ov;
      }
    }
  }
}
__global__ __launch_bounds__(512) void k3_gemm2(G2Args a) {
  extern __shared__ __attribute__((aligned(16))) char smem_[];
  LAS char* smem = (LAS char*)smem_;
  const int wid = __builtin_amdgcn_readfirstlane(threadIdx.x >> 6), lane = threadIdx.x & 63;
  const int b = blockIdx.x;
  const int id = (b & 7) * 32 + (b >> 3);
  const int st = id >> 4, ks = (id >> 2) & 3, dtile = id & 3;
  const int d0 = dtile * 144, s0 = st * 128, tile = st * 4 + dtile;
  switch (wid) {
    case 0: case 1: g2_body<5, 4, 0>(a, smem, wid, lane, d0, s0, ks, 0, tile); break;
    case 2: case 3: g2_body<4, 4, 0>(a, smem, wid, lane, d0, s0, ks, 5, tile); break;
    case 4: case 5: g2_body<5, 5, 1>(a, smem, wid - 4, lane, d0, s0, ks, 0, tile); break;
    default: g2_body<4, 4, 1>(a, smem, wid - 4, lane, d0, s0, ks, 5, tile); break;
  }
}

#define RUNK(k)
extern "C" void kernel_launch(void* const* d_in, const int* in_sizes, int n_in, void* d_out, int out_size, void* d_ws, size_t ws_size,
                              hipStream_t stream) {
  const bf16_t* x = (const bf16_t*)d_in[0];
  const bf16_t* logA = (const bf16_t*)d_in[1];
  const bf16_t* Aim = (const bf16_t*)d_in[2];
  const bf16_t* Cre = (const bf16_t*)d_in[3];
  const bf16_t* Cim = (const bf16_t*)d_in[4];
  const bf16_t* logdt = (const bf16_t*)d_in[5];
  const bf16_t* Dskip = (const bf16_t*)d_in[6];
  const bf16_t* w1 = (const bf16_t*)d_in[8];
  const bf16_t* b1 = (const bf16_t*)d_in[9];
  const bf16_t* w2 = (const bf16_t*)d_in[10];
  const bf16_t* b2 = (const bf16_t*)d_in[11];
  const bf16_t* gate_w = (const bf16_t*)d_in[12];
  char* ws = (char*)d_ws;
  bf16_t* hT = (bf16_t*)(ws + WS_HT);
  bf16_t* aT = (bf16_t*)(ws + WS_AT);
  float* gate = (float*)(ws + WS_G);
  bf16_t* u = (bf16_t*)(ws + WS_U);
  float* slab = (float*)(ws + WS_P);
  unsigned* cnt = (unsigned*)(ws + WS_CNT);
  (void)hipFuncSetAttribute((const void*)k1_prep, hipFuncAttributeMaxDynamicSharedMemorySize, K1_LDS);
  (void)hipFuncSetAttribute((const void*)k2_gemm1, hipFuncAttributeMaxDynamicSharedMemorySize, K2_LDS);
  (void)hipFuncSetAttribute((const void*)k3_gemm2, hipFuncAttributeMaxDynamicSharedMemorySize, G2_LDS);
  SsmArgs a1 = {x, logA, Aim, Cre, Cim, logdt, Dskip, gate_w, hT, aT, gate, w1, cnt};
  SsmArgs a2 = {x, logA, Aim, Cre, Cim, logdt, Dskip, gate_w, hT, aT, gate, w2, cnt};
  RUNK(0) k1_prep<<<256, 512, K1_LDS, stream>>>(a1);
  RUNK(1) k2_gemm1<<<216 + 32, 512, K2_LDS, stream>>>(hT, w1, b1, u, a2);
  G2Args g2 = {u, w2, slab, cnt, b2, gate, aT, (bf16_t*)d_out};
  RUNK(2) k3_gemm2<<<256, 512, G2_LDS, stream>>>(g2);
}
```

```cpp
#include <hip/hip_runtime.h>
#include <hip/hip_bf16.h>
#include <stdint.h>

typedef unsigned short bf16_t;
typedef short bf16x8 __attribute__((ext_vector_type(8)));
typedef short bf16x4 __attribute__((ext_vector_type(4)));
typedef float f32x4 __attribute__((ext_vector_type(4)));
typedef unsigned u32x4 __attribute__((ext_vector_type(4)));
typedef unsigned u32x2 __attribute__((ext_vector_type(2)));
#define LAS __attribute__((address_space(3)))

constexpr int L_ = 2048, D_ = 576, NM_ = 128, F_ = 4608;
constexpr int HT_LD = 2048, U_LD = 4608;

__device__ __forceinline__ float bf2f(bf16_t v) { return __uint_as_float(((unsigned)v) << 16); }
__device__ __forceinline__ bf16_t f2bf(float f) {
  __hip_bfloat16 h = __float2bfloat16(f);
  return __builtin_bit_cast(unsigned short, h);
}
__device__ __forceinline__ float rbf(float f) { return bf2f(f2bf(f)); }
__device__ __forceinline__ unsigned pack2(float lo, float hi) { unsigned r; asm("v_cvt_pk_bf16_f32 %0, %1, %2" : "=v"(r) : "v"(lo), "v"(hi)); return r; }
typedef float f32x2 __attribute__((ext_vector_type(2)));

#define STAMP_AT(i)
#define SSTAMP(i)
#define GSTAMP(i) STAMP_AT(i)
#define HSTAMP(i)
constexpr size_t WS_HT = 0;
constexpr size_t WS_AT = WS_HT + (size_t)D_ * HT_LD * 2;
constexpr size_t WS_G  = WS_AT + (size_t)D_ * L_ * 2;
constexpr size_t WS_U  = WS_G + (size_t)L_ * 4;
constexpr size_t WS_CNT = WS_U + (size_t)D_ * U_LD * 2;
constexpr size_t WS_P  = WS_CNT + 64 * 64 * 4;

constexpr int TB_STR = 528;
constexpr int SS_UL = 0;
constexpr int SS_KN = SS_UL + 4224;
constexpr int SS_XH = SS_KN + 2624;
constexpr int SS_XL = SS_XH + 41 * TB_STR;
constexpr int SS_YH = SS_XL + 41 * TB_STR;
constexpr int SS_YL = SS_YH + 16 * TB_STR;
constexpr int SS_TEND = SS_YL + 16 * TB_STR + 7 * TB_STR;
constexpr int SS_KH = SS_XH;
constexpr int SS_KL = SS_KH + 4608;
constexpr int SS_V = SS_KL + 4608;
constexpr int SS_VSTR = 4160;
constexpr int SS_END = SS_TEND;
static_assert(SS_V + 8 * SS_VSTR <= SS_END, "alias");
constexpr int SS_LN = 2 * SS_END;
constexpr int LN_STRIDE = 1152 + 16;
constexpr int SS_PF = SS_LN + 8 * LN_STRIDE;
constexpr int K1_LDS = SS_PF + 2048;

typedef float f32x2v __attribute__((ext_vector_type(2)));
__device__ __forceinline__ int prow_of(int r) {
  if (r < 16) return 128 * (r >> 3) + (r & 7);
  if (r < 24) return 256 + 128 * ((r - 16) >> 2) + 2 * ((r - 16) & 3);
  if (r < 32) return 512 + 128 * ((r - 24) >> 1) + 4 * ((r - 24) & 1);
  if (r <= 40) return 1024 + 128 * (r - 32);
  return 0;
}
__device__ __forceinline__ int pos_rank(int pos) {
  const int b = (int)(__float_as_uint((float)pos) >> 16) - 0x4380 + 256;
  return pos < 256 ? pos : b;
}
__device__ __forceinline__ f32x2 cexp_p(float are2, float aih, float ail, float p) {
  const float mag = __builtin_amdgcn_exp2f(are2 * p);
  const float pr = aih * p;
  const float er2 = __builtin_fmaf(aih, p, -pr);
  const float rr = __builtin_amdgcn_fractf(pr) + __builtin_fmaf(ail, p, er2);
  return (f32x2){__builtin_amdgcn_cosf(rr), __builtin_amdgcn_sinf(rr)} * mag;
}
__device__ __forceinline__ f32x2 cmul(f32x2 a, f32x2 b) {
  const f32x2 t = (f32x2){a[0], a[0]} * b;
  return __builtin_elementwise_fma((f32x2){a[1], a[1]}, (f32x2){-b[1], b[0]}, t);
}
__device__ __forceinline__ void split2(float a, float b, unsigned& hi, unsigned& lo) {
  hi = pack2(a, b);
  const f32x2 r = (f32x2){a, b} - (f32x2){__uint_as_float(hi << 16), __uint_as_float(hi & 0xffff0000u)};
  lo = pack2(r[0], r[1]);
}
__device__ __forceinline__ float wave_sum(float v) {
  v += __builtin_bit_cast(float, __builtin_amdgcn_update_dpp(0, __builtin_bit_cast(int, v), 0xB1, 0xF, 0xF, true));
  v += __builtin_bit_cast(float, __builtin_amdgcn_update_dpp(0, __builtin_bit_cast(int, v), 0x4E, 0xF, 0xF, true));
  v += __builtin_bit_cast(float, __builtin_amdgcn_update_dpp(0, __builtin_bit_cast(int, v), 0x141, 0xF, 0xF, true));
  v += __builtin_bit_cast(float, __builtin_amdgcn_update_dpp(0, __builtin_bit_cast(int, v), 0x140, 0xF, 0xF, true));
  const float r0 = __builtin_bit_cast(float, __builtin_amdgcn_readlane(__builtin_bit_cast(int, v), 0));
  const float r1 = __builtin_bit_cast(float, __builtin_amdgcn_readlane(__builtin_bit_cast(int, v), 16));
  const float r2 = __builtin_bit_cast(float, __builtin_amdgcn_readlane(__builtin_bit_cast(int, v), 32));
  const float r3 = __builtin_bit_cast(float, __builtin_amdgcn_readlane(__builtin_bit_cast(int, v), 48));
  return (r0 + r1) + (r2 + r3);
}

constexpr size_t SLAB_BYTES = (size_t)64 * 4 * 4 * 8 * 3 * 1024;
struct SsmArgs {
  const bf16_t* x; const bf16_t* logA; const bf16_t* Aim; const bf16_t* Cre; const bf16_t* Cim; const bf16_t* logdt; const bf16_t* Dskip;
  const bf16_t* gate_w; bf16_t* hT; bf16_t* aT; float* gate; const bf16_t* pf;
  unsigned* cnt;
};

template <int ROWBYTES, int ROWS>
__device__ __forceinline__ void prefetch_rows(const char* base, size_t pitch, LAS char* scratch, int tid) {
  constexpr int SEG = ROWBYTES / 64;
  constexpr int TOT = SEG * ROWS;
#pragma unroll 1
  for (int i = tid; i < TOT; i += 512) {
    const int r = i / SEG, sgm = i - r * SEG;
    __builtin_amdgcn_global_load_lds((const unsigned*)(base + (size_t)r * pitch + sgm * 64), (LAS unsigned*)(scratch + (tid >> 6) * 256), 4, 0, 0);
  }
}

#define LDS_BARRIER() do { asm volatile("" ::: "memory"); __builtin_amdgcn_s_waitcnt(0xC07F); __builtin_amdgcn_s_barrier(); asm volatile("" ::: "memory"); } while (0)
template <bool DO_LN>
__device__ __forceinline__ void ssm_wg(const SsmArgs& a, int d0, int lnrow0, const char* pfbase, size_t pfpitch, LAS char* smem_all) {
  const int tid = threadIdx.x, wid8 = __builtin_amdgcn_readfirstlane(tid >> 6), lane = tid & 63;
  const int half = wid8 >> 2, wid = wid8 & 3, t256 = tid & 255;
  const int d = d0 + half;
  LAS char* smem = smem_all + half * SS_END;
  SSTAMP(0);
  const int mode = 64 * (wid & 1) + lane;
  const int mi = d * NM_ + mode;
  const bf16_t r_dt = a.logdt[d], r_ds = a.Dskip[d], r_la = a.logA[mi], r_ai = a.Aim[mi], r_cr = a.Cre[mi], r_ci = a.Cim[mi];
  __builtin_amdgcn_sched_barrier(0);
  u32x2 lnv[3], lnw[3];
  if (DO_LN) {
    const bf16_t* xr = a.x + (size_t)(lnrow0 + wid8) * D_;
#pragma unroll
    for (int k = 0; k < 3; ++k) {
      const int ch = (k < 2 || lane < 16) ? lane + 64 * k : 0;
      lnv[k] = *(const u32x2*)(xr + ch * 4);
      lnw[k] = *(const u32x2*)(a.gate_w + ch * 4);
    }
  }
  unsigned ucol[4];
#pragma unroll
  for (int k = 0; k < 4; ++k) ucol[k] = *(const unsigned*)(a.x + (size_t)(tid + 512 * k) * D_ + d0);
  __builtin_amdgcn_sched_barrier(0);
  const float dt = rbf(expf(bf2f(r_dt)));
  const float dsk = bf2f(r_ds);
  {
    const float are2 = -rbf(expf(bf2f(r_la))) * dt * 1.44269504088896340736f;
    const float aim = bf2f(r_ai) * dt;
    const float I2H = 0.159154936671257019043f;
    const float I2L = 6.42063824376987e-09f;
    const float aih = aim * I2H;
    const float ail = __builtin_fmaf(aim, I2H, -aih) + aim * I2L;
    LAS char* xh = smem + SS_XH + mode * 4;
    LAS char* xl = smem + SS_XL + mode * 4;
    auto put = [&](int row, f32x2 v) {
      unsigned h_, l_; split2(v[0], v[1], h_, l_);
      *(LAS unsigned*)(xh + row * TB_STR) = h_;
      *(LAS unsigned*)(xl + row * TB_STR) = l_;
    };
    const f32x2 z128 = cexp_p(are2, aih, ail, 128.f);
    if (wid < 2) {
      const f32x2 z1 = cexp_p(are2, aih, ail, 1.f);
      const f32x2 z8 = cexp_p(are2, aih, ail, 8.f);
      f32x2 e = {1.f, 0.f};
#pragma unroll
      for (int k = 0; k < 8; ++k) {
        put(k, e);
        put(8 + k, k ? cmul(z128, e) : z128);
        if (k < 7) e = k ? cmul(e, z1) : z1;
      }
      f32x2 y = {2.0f * bf2f(r_cr) * dt, 2.0f * bf2f(r_ci) * dt};
#pragma unroll
      for (int j = 0; j < 16; ++j) {
        unsigned h_, l_; split2(y[0], -y[1], h_, l_);
        *(LAS unsigned*)(smem + SS_YH + mode * 4 + j * TB_STR) = h_;
        *(LAS unsigned*)(smem + SS_YL + mode * 4 + j * TB_STR) = l_;
        if (j < 15) y = cmul(y, z8);
      }
    } else {
      const f32x2 z2 = cexp_p(are2, aih, ail, 2.f);
      const f32x2 z4 = cmul(z2, z2), z6 = cmul(z4, z2);
      f32x2 w = cmul(z128, z128);
#pragma unroll
      for (int i = 2; i <= 16; ++i) {
        if (i < 4) { const int r = 16 + 4 * (i - 2); put(r, w); put(r + 1, cmul(w, z2)); put(r + 2, cmul(w, z4)); put(r + 3, cmul(w, z6)); }
        else if (i < 8) { const int r = 24 + 2 * (i - 4); put(r, w); put(r + 1, cmul(w, z4)); }
        else put(32 + (i - 8), w);
        if (i < 16) w = cmul(w, z128);
      }
    }
  }
  SSTAMP(1);
  if (t256 < 16) *(LAS bf16_t*)(smem + SS_UL + t256 * 2) = 0;
  if (t256 < 48) *(LAS bf16_t*)(smem + SS_UL + (16 + 2048 + t256) * 2) = 0;
#pragma unroll
  for (int k = 0; k < 4; ++k) {
    *(LAS bf16_t*)(smem_all + SS_UL + (16 + tid + 512 * k) * 2) = (bf16_t)(ucol[k] & 0xffffu);
    *(LAS bf16_t*)(smem_all + SS_END + SS_UL + (16 + tid + 512 * k) * 2) = (bf16_t)(ucol[k] >> 16);
  }
  if (DO_LN) {
    float xv[12], sum = 0.f, gs = 0.f;
#pragma unroll
    for (int k = 0; k < 3; ++k) {
      const bool ok = (k < 2 || lane < 16);
#pragma unroll
      for (int j = 0; j < 2; ++j) {
        const float a0 = ok ? __uint_as_float(lnv[k][j] << 16) : 0.f, a1 = ok ? __uint_as_float(lnv[k][j] & 0xffff0000u) : 0.f;
        const float w0 = __uint_as_float(lnw[k][j] << 16), w1 = __uint_as_float(lnw[k][j] & 0xffff0000u);
        xv[4 * k + 2 * j] = a0; xv[4 * k + 2 * j + 1] = a1;
        sum += a0 + a1;
        gs += rbf(a0 * w0) + rbf(a1 * w1);
      }
    }
    sum = wave_sum(sum); gs = wave_sum(gs);
    const float mean = sum * (1.0f / D_);
    float vs = 0.f;
#pragma unroll
    for (int k = 0; k < 3; ++k)
#pragma unroll
      for (int j = 0; j < 4; ++j) { const float dd = xv[4 * k + j] - mean; vs += (k < 2 || lane < 16) ? dd * dd : 0.f; }
    vs = wave_sum(vs);
    const float mb = rbf(mean);
    const float vb = rbf(vs * (1.0f / D_));
    const float den = rbf(sqrtf(rbf(vb + 1e-5f)));
    if (lane == 0) a.gate[lnrow0 + wid8] = rbf(1.0f / (1.0f + expf(-rbf(gs))));
#pragma unroll
    for (int k = 0; k < 3; ++k)
      if (k < 2 || lane < 16) {
        u32x2 o = {pack2(rbf(xv[4 * k] - mb) / den, rbf(xv[4 * k + 1] - mb) / den), pack2(rbf(xv[4 * k + 2] - mb) / den, rbf(xv[4 * k + 3] - mb) / den)};
        *(LAS u32x2*)(smem_all + SS_LN + wid8 * LN_STRIDE + (lane + 64 * k) * 8) = o;
      }
  }
  SSTAMP(2);
  if (DO_LN && blockIdx.x < 64 && tid == 0) a.cnt[blockIdx.x * 64] = 0u;
  if (DO_LN) prefetch_rows<1152, 64>(pfbase, pfpitch, smem_all + SS_PF, tid);
  else prefetch_rows<512, 1152>(pfbase, pfpitch, smem_all + SS_PF, tid);
  LDS_BARRIER();
  SSTAMP(3);
  if (wid < 3) {
    const int i = lane & 15, g = lane >> 4;
    f32x4 acc = {0, 0, 0, 0};
    const LAS char* xa = smem + (16 * wid + i) * TB_STR + g * 16;
    const LAS char* ya = smem + i * TB_STR + g * 16;
#pragma unroll
    for (int t = 0; t < 8; ++t) {
      const bf16x8 XH = *(const LAS bf16x8*)(xa + SS_XH + t * 64), XL = *(const LAS bf16x8*)(xa + SS_XL + t * 64);
      const bf16x8 YH = *(const LAS bf16x8*)(ya + SS_YH + t * 64), YL = *(const LAS bf16x8*)(ya + SS_YL + t * 64);
      acc = __builtin_amdgcn_mfma_f32_16x16x32_bf16(XL, YH, acc, 0, 0, 0);
      acc = __builtin_amdgcn_mfma_f32_16x16x32_bf16(XH, YL, acc, 0, 0, 0);
      acc = __builtin_amdgcn_mfma_f32_16x16x32_bf16(XH, YH, acc, 0, 0, 0);
    }
#pragma unroll
    for (int j = 0; j < 4; ++j) {
      const int row = 16 * wid + 4 * g + j;
      if (row < 40 || (row == 40 && i == 0)) *(LAS float*)(smem + SS_KN + pos_rank(prow_of(row) + 8 * i) * 4) = acc[j];
    }
  } else if (DO_LN) {
    const int t128 = (half * 64 + lane);
#pragma unroll
    for (int k = 0; k < 5; ++k) {
      const int dd = t128 + 128 * k;
      if (dd < D_) {
        unsigned v[8];
#pragma unroll
        for (int r = 0; r < 8; ++r) v[r] = *(const LAS bf16_t*)(smem_all + SS_LN + r * LN_STRIDE + dd * 2);
        u32x4 o = {v[0] | (v[1] << 16), v[2] | (v[3] << 16), v[4] | (v[5] << 16), v[6] | (v[7] << 16)};
        *(u32x4*)(a.hT + (size_t)dd * HT_LD + lnrow0) = o;
      }
    }
  }
  LDS_BARRIER();
  SSTAMP(4);
  {
    u32x4 oh, ol;
#pragma unroll
    for (int e = 0; e < 8; e += 2) {
      const int i0 = 2047 - (t256 * 8 + e), i1 = i0 - 1;
      const unsigned pb = pack2((float)i0, (float)i1);
      const int k0 = i0 < 256 ? i0 : (int)(pb & 0xffffu) - 0x4380 + 256;
      const int k1 = i1 < 256 ? i1 : (int)(pb >> 16) - 0x4380 + 256;
      const float v0 = *(const LAS float*)(smem + SS_KN + k0 * 4), v1 = *(const LAS float*)(smem + SS_KN + k1 * 4);
      unsigned h_, l_; split2(v0, v1, h_, l_);
      oh[e >> 1] = h_; ol[e >> 1] = l_;
    }
    *(LAS u32x4*)(smem + SS_KH + t256 * 16) = oh;
    *(LAS u32x4*)(smem + SS_KL + t256 * 16) = ol;
    if (t256 < 32) {
      *(LAS u32x4*)(smem + SS_KH + (256 + t256) * 16) = (u32x4){0, 0, 0, 0};
      *(LAS u32x4*)(smem + SS_KL + (256 + t256) * 16) = (u32x4){0, 0, 0, 0};
    }
  }
  {
    const u32x4 lo = *(const LAS u32x4*)(smem + SS_UL + t256 * 16);
    const u32x4 hi = *(const LAS u32x4*)(smem + SS_UL + t256 * 16 + 16);
    unsigned w[8] = {lo[0], lo[1], lo[2], lo[3], hi[0], hi[1], hi[2], hi[3]};
#pragma unroll
    for (int rho = 0; rho < 8; ++rho) {
      u32x4 o;
#pragma unroll
      for (int j = 0; j < 4; ++j) {
        const int b = (rho >> 1) + j;
        o[j] = (rho & 1) ? __builtin_amdgcn_alignbit(w[b + 1], w[b], 16) : w[b];
      }
      *(LAS u32x4*)(smem + SS_V + rho * SS_VSTR + t256 * 16) = o;
    }
    if (t256 < 32) {
      const int ch = 256 + (t256 & 3), rho = t256 >> 2;
      const LAS char* src = smem + SS_UL + ch * 16 + (rho >> 1) * 4;
      unsigned q[5];
#pragma unroll
      for (int j = 0; j < 5; ++j) q[j] = *(const LAS unsigned*)(src + j * 4);
      u32x4 o;
#pragma unroll
      for (int j = 0; j < 4; ++j) o[j] = (rho & 1) ? __builtin_amdgcn_alignbit(q[j + 1], q[j], 16) : q[j];
      *(LAS u32x4*)(smem + SS_V + rho * SS_VSTR + ch * 16) = o;
    }
  }
  LDS_BARRIER();
  SSTAMP(5);
  f32x4 acc[8];
#pragma unroll
  for (int r = 0; r < 8; ++r) acc[r] = (f32x4){0, 0, 0, 0};
  {
    const int ij = lane & 15, g = lane >> 4;
    const int rho = (ij + 1) & 7, h = (ij + 1) >> 3;
    const LAS char* gbase = smem + SS_V + rho * SS_VSTR + (8 * g + 8 * h) * 2;
    const int kofs = (2032 - 16 * ij + 8 * g) * 2;
#pragma unroll 1
    for (int cc = 0; cc < 2; ++cc) {
      const int c = 2 * wid + cc;
      bf16x8 G[8];
#pragma unroll
      for (int b = 0; b < 8; ++b) G[b] = *(const LAS bf16x8*)(gbase + (256 * b + 32 * c) * 2);
#pragma unroll
      for (int aa = 0; aa < 8; ++aa) {
        const bf16x8 fh = *(const LAS bf16x8*)(smem + SS_KH + kofs + (32 * c - 256 * aa) * 2);
        const bf16x8 fl = *(const LAS bf16x8*)(smem + SS_KL + kofs + (32 * c - 256 * aa) * 2);
#pragma unroll
        for (int b = 0; b + aa < 8; ++b) {
          acc[aa + b] = __builtin_amdgcn_mfma_f32_16x16x32_bf16(G[b], fh, acc[aa + b], 0, 0, 0);
          acc[aa + b] = __builtin_amdgcn_mfma_f32_16x16x32_bf16(G[b], fl, acc[aa + b], 0, 0, 0);
        }
      }
    }
  }
  SSTAMP(6);
  LDS_BARRIER();
#pragma unroll
  for (int r = 0; r < 8; ++r) *(LAS f32x4*)(smem + SS_V + (wid * 8 + r) * 1024 + lane * 16) = acc[r];
  LDS_BARRIER();
#pragma unroll
  for (int rr = 0; rr < 2; ++rr) {
    const int r = 2 * wid + rr;
    f32x4 s = *(const LAS f32x4*)(smem + SS_V + r * 1024 + lane * 16);
#pragma unroll
    for (int w = 1; w < 4; ++w) s += *(const LAS f32x4*)(smem + SS_V + (w * 8 + r) * 1024 + lane * 16);
    const int l0 = 256 * r + 16 * (lane & 15) + 4 * (lane >> 4);
    const bf16x4 uu = *(const LAS bf16x4*)(smem + SS_UL + (16 + l0) * 2);
    float o[4];
#pragma unroll
    for (int j = 0; j < 4; ++j) o[j] = rbf(s[j]) + rbf(bf2f((bf16_t)uu[j]) * dsk);
    u32x2 ov = {pack2(o[0], o[1]), pack2(o[2], o[3])};
    *(u32x2*)(a.aT + (size_t)d * L_ + l0) = ov;
  }
  asm volatile("s_waitcnt vmcnt(0)" ::: "memory");
  SSTAMP(7);
}

__global__ __launch_bounds__(512) void k1_prep(SsmArgs a) {
  extern __shared__ __attribute__((aligned(16))) char smem_[];
  const int b = blockIdx.x;
  const int rb = (b & 7) * 32 + (b >> 3);
  const char* pf = (const char*)a.pf + (size_t)(b >> 3) * 64 * (F_ * 2) + (b & 7) * 1152;
  ssm_wg<true>(a, 2 * b, 8 * rb, pf, (size_t)F_ * 2, (LAS char*)smem_);
}

template <int AB> struct GemmCfg {
  static constexpr int ACT_BYTES = AB * 16 * 128;
  static constexpr int STAGE = ACT_BYTES + 64 * 256;
};

#define WAIT_VM(n) asm volatile("s_waitcnt vmcnt(%0)" ::"n"(n) : "memory")

template <int N> __device__ __forceinline__ void wait_vm() { asm volatile("s_waitcnt vmcnt(%0)" ::"n"(N) : "memory"); }

template <int AB, int ABW, int NG, int SW, int SA, int NT, bool ACT_A, int LDA, int LDW, int GRP>
__device__ __forceinline__ void gemm_wave(const bf16_t* __restrict__ actg,
                                          const bf16_t* __restrict__ wg,
                                          LAS char* smem, int w4, int lane, int ab0, f32x4 (&acc)[ABW][4]) {
  constexpr int ACTB = AB * 2048, WB = 16384, ABASE = SW * WB;
  constexpr int S = GRP == 0 ? SW : SA;
  constexpr int SMAX = SW > SA ? SW : SA;
  const int ww = w4 & 1;
  const char* gp;
  if (GRP == 0) {
    const int wf = (lane >> 4) | (((w4 >> 1) & 1) << 2);
    gp = (const char*)wg + (unsigned)(((lane >> 4) + 4 * w4) * LDW + (((lane & 15) ^ (wf << 1)) * 8)) * 2u;
  } else {
    gp = (const char*)actg + (unsigned)(((lane >> 3) + 8 * w4) * LDA + (((lane & 7) ^ (lane >> 3)) * 8)) * 2u;
  }
  auto issue = [&](int t, int buf) {
    LAS char* sb = smem + (GRP == 0 ? buf * WB : ABASE + buf * ACTB) + w4 * 1024;
    const char* g = gp + (size_t)t * (GRP == 0 ? (size_t)64 * LDW * 2 : (size_t)128);
#pragma unroll
    for (int j = 0; j < NG; ++j)
      __builtin_amdgcn_global_load_lds((const unsigned*)(g + (size_t)j * (GRP == 0 ? 16 * LDW * 2 : 32 * LDA * 2)), (LAS unsigned*)(sb + j * 4096), 16, 0, 0);
  };
  const int c = lane & 15, g = lane >> 4;
  const int ard = ABASE + (16 * ab0 + c) * 128 + (((4 * GRP + g) ^ (c & 7)) * 16);
  const int q4 = (lane & 15) >> 2, p4 = lane & 3;
  const int ff = q4 | ((g & 1) << 2);
  int wrd[4];
#pragma unroll
  for (int nb = 0; nb < 4; ++nb) wrd[nb] = GRP * 8192 + (8 * g + q4) * 256 + (((4 * ww + nb) ^ ff) * 32) + p4 * 8;

  bf16x8 fa0[ABW], fa1[ABW];
  bf16x4 fw0[8], fw1[8];
  auto step = [&](bf16x8 (&faM)[ABW], bf16x4 (&fwM)[8], bf16x8 (&faR)[ABW], bf16x4 (&fwR)[8], int bw, int ba, bool doRead, int tI, int bI, bool doIssue) {
    const LAS char* sw = smem + bw * WB;
    const LAS char* sa = smem + ba * ACTB;
    LAS char* sb = smem + (GRP == 0 ? bI * WB : ABASE + bI * ACTB) + w4 * 1024;
    const char* gsrc = gp + (size_t)tI * (GRP == 0 ? (size_t)64 * LDW * 2 : (size_t)128);
    constexpr int NR = 8 + ABW, NM = 4 * ABW;
#pragma unroll
    for (int z = 0; z < NM; ++z) {
      const int r0 = (NM >= NR + 4) ? z : (3 * z + 1) / 2, r1 = (NM >= NR + 4) ? z + 1 : (3 * (z + 1) + 1) / 2;
      if (doRead) {
#pragma unroll
        for (int r = r0; r < r1; ++r) {
          if (r < 8) fwR[r] = __builtin_amdgcn_ds_read_tr16_b64_v4i16((LAS bf16x4*)(sw + wrd[r >> 1] + (r & 1) * 1024));
          else if (r < NR) faR[r - 8] = *(const LAS bf16x8*)(sa + ard + (r - 8) * 2048);
        }
      }
      {
        const int ab = z >> 2, nb = z & 3;
        const bf16x8 wv = __builtin_shufflevector(fwM[2 * nb], fwM[2 * nb + 1], 0, 1, 2, 3, 4, 5, 6, 7);
        acc[ab][nb] = ACT_A ? __builtin_amdgcn_mfma_f32_16x16x32_bf16(faM[ab], wv, acc[ab][nb], 0, 0, 0)
                            : __builtin_amdgcn_mfma_f32_16x16x32_bf16(wv, faM[ab], acc[ab][nb], 0, 0, 0);
      }
      if (doIssue && z >= NM - NG - 1 && z < NM - 1) {
        const int j = z - (NM - NG - 1);
        __builtin_amdgcn_global_load_lds((const unsigned*)(gsrc + (size_t)j * (GRP == 0 ? 16 * LDW * 2 : 32 * LDA * 2)), (LAS unsigned*)(sb + j * 4096), 16, 0, 0);
      }
      __builtin_amdgcn_sched_barrier(0);
    }
  };
#define BARRIER() do { asm volatile("" ::: "memory"); __builtin_amdgcn_s_waitcnt(0xC07F); __builtin_amdgcn_s_barrier(); asm volatile("" ::: "memory"); } while (0)
#define SCHED() __builtin_amdgcn_sched_barrier(0)
#pragma unroll
  for (int z = 0; z < 8; ++z) fw1[z] = (bf16x4){0, 0, 0, 0};
#pragma unroll
  for (int ab = 0; ab < ABW; ++ab) fa1[ab] = (bf16x8){0, 0, 0, 0, 0, 0, 0, 0};
#pragma unroll
  for (int s = 0; s < S - 1; ++s) issue(s, s);
  int bw = 0, ba = 0, ibuf = S - 1;
  auto adv = [&]() {
    ibuf = (ibuf + 1 == S) ? 0 : ibuf + 1;
    bw = (bw + 1 == SW) ? 0 : bw + 1;
    ba = (ba + 1 == SA) ? 0 : ba + 1;
  };
  constexpr int NMAIN = ((NT - (SMAX - 1)) / 2) * 2;
#pragma unroll 1
  for (int t = 0; t < NMAIN; t += 2) {
    wait_vm<(S - 2) * NG>();
    BARRIER();
    SCHED();
    if (!ACT_A && t == 0) GSTAMP(1);
    if (ACT_A && t == 0) HSTAMP(1);
    step(fa1, fw1, fa0, fw0, bw, ba, true, t + S - 1, ibuf, true);
    adv();
    wait_vm<(S - 2) * NG>();
    BARRIER();
    SCHED();
    step(fa0, fw0, fa1, fw1, bw, ba, true, t + S, ibuf, true);
    adv();
  }
#pragma unroll
  for (int t = NMAIN; t < NT; ++t) {
    switch ((t + S - 2 < NT - 1 ? S - 2 : NT - 1 - t)) {
      case 0: wait_vm<0>(); break;
      case 1: wait_vm<1 * NG>(); break;
      case 2: wait_vm<2 * NG>(); break;
      default: wait_vm<3 * NG>(); break;
    }
    BARRIER();
    SCHED();
    if ((t & 1) == 0) step(fa1, fw1, fa0, fw0, bw, ba, true, t + S - 1, ibuf, t + S - 1 < NT);
    else step(fa0, fw0, fa1, fw1, bw, ba, true, t + S - 1, ibuf, t + S - 1 < NT);
    adv();
  }
  __builtin_amdgcn_s_waitcnt(0xC07F);
  if ((NT & 1) == 0) step(fa1, fw1, fa0, fw0, 0, 0, false, 0, 0, false); else step(fa0, fw0, fa1, fw1, 0, 0, false, 0, 0, false);
}

__device__ __forceinline__ float gelu_tanh(float x) {
  const float c1 = -2.0f * 0.7978845608028654f * 1.4426950408889634f;
  const float c2 = c1 * 0.044715f;
  const float e = __builtin_amdgcn_exp2f(x * __builtin_fmaf(c2, x * x, c1));
  return x * __builtin_amdgcn_rcpf(1.0f + e);
}

template <int ABW, int GRP, int RSTR>
__device__ __forceinline__ void pair_reduce(f32x4 (&acc)[ABW][4], LAS char* smem, int w4, int lane) {
  __syncthreads();
  LAS char* mine = smem + (GRP * 4 + w4) * RSTR + lane * 16;
  const LAS char* theirs = smem + ((1 - GRP) * 4 + w4) * RSTR + lane * 16;
#pragma unroll
  for (int ab = 0; ab < ABW; ++ab)
#pragma unroll
    for (int h = 0; h < 2; ++h) *(LAS f32x4*)(mine + (ab * 2 + h) * 1024) = acc[ab][(GRP ? 0 : 2) + h];
  __syncthreads();
#pragma unroll
  for (int ab = 0; ab < ABW; ++ab)
#pragma unroll
    for (int h = 0; h < 2; ++h) acc[ab][(GRP ? 2 : 0) + h] += *(const LAS f32x4*)(theirs + (ab * 2 + h) * 1024);
}

#define G1SW 4
#define G1SA 4
constexpr int G1_LDS = G1SW * 16384 + G1SA * 6 * 2048;
template <int GRP>
__device__ __forceinline__ void g1_body(const bf16_t* __restrict__ hT, const bf16_t* __restrict__ w1, const bf16_t* __restrict__ b1,
                                        bf16_t* __restrict__ u, LAS char* smem, int w4, int lane, int d0, int n0) {
  f32x4 acc[3][4];
#pragma unroll
  for (int a = 0; a < 3; ++a)
#pragma unroll
    for (int n = 0; n < 4; ++n) acc[a][n] = (f32x4){0, 0, 0, 0};
  const int wa = w4 >> 1, ww = w4 & 1;
  gemm_wave<6, 3, GRP == 0 ? 4 : 3, G1SW, G1SA, 32, false, HT_LD, F_, GRP>(hT + (size_t)d0 * HT_LD, w1 + n0, smem, w4, lane, 3 * wa, acc);
  GSTAMP(2);
  pair_reduce<3, GRP, 6144>(acc, smem, w4, lane);
  const int fq = lane >> 4, c = lane & 15;
#pragma unroll
  for (int h = 0; h < 2; ++h) {
    const int nb = 2 * GRP + h;
    const int f = n0 + 64 * ww + 16 * nb + 4 * fq;
    const bf16x4 bv = *(const bf16x4*)(b1 + f);
#pragma unroll
    for (int ab = 0; ab < 3; ++ab) {
      const int d = d0 + 48 * wa + 16 * ab + c;
      float o[4];
#pragma unroll
      for (int j = 0; j < 4; ++j) o[j] = gelu_tanh(rbf(rbf(acc[ab][nb][j]) + bf2f((bf16_t)bv[j])));
      u32x2 ov = {pack2(o[0], o[1]), pack2(o[2], o[3])};
      *(u32x2*)(u + (size_t)d * U_LD + f) = ov;
    }
  }
}
constexpr int K2_LDS = G1_LDS > K1_LDS ? G1_LDS : K1_LDS;
__global__ __launch_bounds__(512) void k2_gemm1(const bf16_t* __restrict__ hT, const bf16_t* __restrict__ w1, const bf16_t* __restrict__ b1,
                                                bf16_t* __restrict__ u, SsmArgs a) {
  extern __shared__ __attribute__((aligned(16))) char smem_[];
  LAS char* smem = (LAS char*)smem_;
  const int wid = __builtin_amdgcn_readfirstlane(threadIdx.x >> 6), lane = threadIdx.x & 63;
  const int b = blockIdx.x;
  if (b >= 216) {
    const int j = b - 216;
    const char* pf = (const char*)a.pf + (size_t)(j >> 3) * 1152 * (L_ * 2) + (j & 7) * 512;
    ssm_wg<false>(a, 512 + 2 * j, 0, pf, (size_t)L_ * 2, smem);
    return;
  }
  const int id = (b & 7) * 27 + (b >> 3);
  const int nt = id / 6, mt = id % 6;
  const int d0 = mt * 96, n0 = nt * 128;
  GSTAMP(0);
  if (wid < 4) g1_body<0>(hT, w1, b1, u, smem, wid, lane, d0, n0);
  else g1_body<1>(hT, w1, b1, u, smem, wid - 4, lane, d0, n0);
  GSTAMP(3);
}

#define G2SW 4
#define G2SA 4
constexpr int G2_LDS = G2SW * 16384 + G2SA * 9 * 2048;
struct G2Args {
  const bf16_t* u; const bf16_t* w2; float* slab; unsigned* cnt; const bf16_t* b2; const float* gate; const bf16_t* aT; bf16_t* out;
};
template <int ABW, int NG, int GRP>
__device__ __forceinline__ void g2_body(const G2Args& a, LAS char* smem, int w4, int lane, int d0, int s0, int ks, int ab0, int tile) {
  f32x4 acc[ABW][4];
#pragma unroll
  for (int x = 0; x < ABW; ++x)
#pragma unroll
    for (int n = 0; n < 4; ++n) acc[x][n] = (f32x4){0, 0, 0, 0};
  float gv[3]; bf16_t bv2r[3], avr[3][4];
  {
    const int fq = lane >> 4, c = lane & 15, ww = w4 & 1;
#pragma unroll
    for (int sl = 0; sl < 3; ++sl) {
      const int e = ks + 4 * sl;
      const int ee = e < 2 * ABW ? e : ks;
      const int s = s0 + 64 * ww + 16 * (2 * GRP + (ee & 1)) + c;
      const int d = d0 + 16 * (ab0 + (ee >> 1)) + 4 * fq;
      gv[sl] = a.gate[s];
      bv2r[sl] = a.b2[s];
#pragma unroll
      for (int j = 0; j < 4; ++j) avr[sl][j] = a.aT[(size_t)(d + j) * L_ + s];
    }
  }
  __builtin_amdgcn_sched_barrier(0);
  gemm_wave<9, ABW, NG, G2SW, G2SA, 18, true, U_LD, L_, GRP>(a.u + (size_t)d0 * U_LD + ks * 1152, a.w2 + (size_t)ks * 1152 * L_ + s0, smem, w4, lane, ab0, acc);
  HSTAMP(2);
  pair_reduce<ABW, GRP, 10240>(acc, smem, w4, lane);
  HSTAMP(3);
  const int fq = lane >> 4, c = lane & 15, ww = w4 & 1, wid8 = GRP * 4 + w4;
  const __amdgpu_buffer_rsrc_t rs = __builtin_amdgcn_make_buffer_rsrc((void*)a.slab, 0, (int)SLAB_BYTES, 0x00020000);
  constexpr int NE = 2 * ABW;
#pragma unroll
  for (int e = 0; e < NE; ++e) {
    const int owner = e & 3;
    if (owner != ks) {
      const int off = ((((tile * 4 + owner) * 4 + ks) * 8 + wid8) * 3 + (e >> 2)) * 1024 + lane * 16;
      __builtin_amdgcn_raw_buffer_store_b128(__builtin_bit_cast(u32x4, acc[e >> 1][2 * GRP + (e & 1)]), rs, off, 0, 16);
    }
  }
  asm volatile("s_waitcnt vmcnt(0)" ::: "memory");
  __syncthreads();
  HSTAMP(4);
  if (wid8 == 0) {
    if (lane == 0) __hip_atomic_fetch_add(a.cnt + tile * 64, 1u, __ATOMIC_RELAXED, __HIP_MEMORY_SCOPE_AGENT);
    unsigned spins = 0;
    while ((unsigned)__builtin_amdgcn_readfirstlane(__hip_atomic_load(a.cnt + tile * 64, __ATOMIC_RELAXED, __HIP_MEMORY_SCOPE_AGENT)) < 4u) {
      __builtin_amdgcn_s_sleep(1);
      if (++spins > (1u << 24)) break;
    }
  }
  asm volatile("" ::: "memory");
  __syncthreads();
  asm volatile("" ::: "memory");
  HSTAMP(5);
  constexpr int NSL = (NE + 3) / 4;
  u32x4 rv[NSL][3];
#pragma unroll
  for (int sl = 0; sl < NSL; ++sl)
#pragma unroll
    for (int k = 1; k < 4; ++k) {
      const int src = (ks + k) & 3;
      const int slc = (ks + 4 * sl < NE) ? sl : 0;
      const int off = ((((tile * 4 + ks) * 4 + src) * 8 + wid8) * 3 + slc) * 1024 + lane * 16;
      rv[sl][k - 1] = __builtin_amdgcn_raw_buffer_load_b128(rs, off, 0, 16);
    }
#pragma unroll
  for (int sl = 0; sl < NSL; ++sl) {
    const int e0 = 4 * sl;
    if (ks + e0 < NE) {
      f32x4 v = {0, 0, 0, 0};
#pragma unroll
      for (int k = 0; k < 4; ++k) if (k == ks && e0 + k < NE) v = acc[(e0 + k) >> 1][2 * GRP + ((e0 + k) & 1)];
#pragma unroll
      for (int k = 0; k < 3; ++k) v += __builtin_bit_cast(f32x4, rv[sl][k]);
      const int e = ks + e0;
      const int s = s0 + 64 * ww + 16 * (2 * GRP + (e & 1)) + c;
      const int d = d0 + 16 * (ab0 + (e >> 1)) + 4 * fq;
      const float g = gv[sl], omg = rbf(1.0f - g);
      float o[4];
#pragma unroll
      for (int j = 0; j < 4; ++j) o[j] = rbf(g * bf2f(avr[sl][j])) + rbf(omg * rbf(rbf(v[j]) + bf2f(bv2r[sl])));
      u32x2 ov = {pack2(o[0], o[1]), pack2(o[2], o[3])};
      *(u32x2*)(a.out + (size_t)s * D_ + d) = ov;
    }
  }
  HSTAMP(6);
}
__global__ __launch_bounds__(512) void k3_gemm2(G2Args a) {
  extern __shared__ __attribute__((aligned(16))) char smem_[];
  LAS char* smem = (LAS char*)smem_;
  const int wid = __builtin_amdgcn_readfirstlane(threadIdx.x >> 6), lane = threadIdx.x & 63;
  const int b = blockIdx.x;
  const int id = (b & 7) * 32 + (b >> 3);
  const int st = id >> 4, ks = (id >> 2) & 3, dtile = id & 3;
  const int d0 = dtile * 144, s0 = st * 128, tile = st * 4 + dtile;
  HSTAMP(0);
  switch (wid) {
    case 0: case 1: g2_body<5, 4, 0>(a, smem, wid, lane, d0, s0, ks, 0, tile); break;
    case 2: case 3: g2_body<4, 4, 0>(a, smem, wid, lane, d0, s0, ks, 5, tile); break;
    case 4: case 5: g2_body<4, 4, 1>(a, smem, wid - 2, lane, d0, s0, ks, 5, tile); break;
    default: g2_body<5, 5, 1>(a, smem, wid - 6, lane, d0, s0, ks, 0, tile); break;
  }
}

#define RUNK(k)
extern "C" void kernel_launch(void* const* d_in, const int* in_sizes, int n_in, void* d_out, int out_size, void* d_ws, size_t ws_size,
                              hipStream_t stream) {
  const bf16_t* x = (const bf16_t*)d_in[0];
  const bf16_t* logA = (const bf16_t*)d_in[1];
  const bf16_t* Aim = (const bf16_t*)d_in[2];
  const bf16_t* Cre = (const bf16_t*)d_in[3];
  const bf16_t* Cim = (const bf16_t*)d_in[4];
  const bf16_t* logdt = (const bf16_t*)d_in[5];
  const bf16_t* Dskip = (const bf16_t*)d_in[6];
  const bf16_t* w1 = (const bf16_t*)d_in[8];
  const bf16_t* b1 = (const bf16_t*)d_in[9];
  const bf16_t* w2 = (const bf16_t*)d_in[10];
  const bf16_t* b2 = (const bf16_t*)d_in[11];
  const bf16_t* gate_w = (const bf16_t*)d_in[12];
  char* ws = (char*)d_ws;
  bf16_t* hT = (bf16_t*)(ws + WS_HT);
  bf16_t* aT = (bf16_t*)(ws + WS_AT);
  float* gate = (float*)(ws + WS_G);
  bf16_t* u = (bf16_t*)(ws + WS_U);
  float* slab = (float*)(ws + WS_P);
  unsigned* cnt = (unsigned*)(ws + WS_CNT);
  (void)hipFuncSetAttribute((const void*)k1_prep, hipFuncAttributeMaxDynamicSharedMemorySize, K1_LDS);
  (void)hipFuncSetAttribute((const void*)k2_gemm1, hipFuncAttributeMaxDynamicSharedMemorySize, K2_LDS);
  (void)hipFuncSetAttribute((const void*)k3_gemm2, hipFuncAttributeMaxDynamicSharedMemorySize, G2_LDS);
  SsmArgs a1 = {x, logA, Aim, Cre, Cim, logdt, Dskip, gate_w, hT, aT, gate, w1, cnt};
  SsmArgs a2 = {x, logA, Aim, Cre, Cim, logdt, Dskip, gate_w, hT, aT, gate, w2, cnt};
  RUNK(0) k1_prep<<<256, 512, K1_LDS, stream>>>(a1);
  RUNK(1) k2_gemm1<<<216 + 32, 512, K2_LDS, stream>>>(hT, w1, b1, u, a2);
  G2Args g2 = {u, w2, slab, cnt, b2, gate, aT, (bf16_t*)d_out};
  RUNK(2) k3_gemm2<<<256, 512, G2_LDS, stream>>>(g2);
}
```

```cpp
#include <hip/hip_runtime.h>
#include <hip/hip_bf16.h>
#include <stdint.h>

typedef unsigned short bf16_t;
typedef short bf16x8 __attribute__((ext_vector_type(8)));
typedef short bf16x4 __attribute__((ext_vector_type(4)));
typedef float f32x4 __attribute__((ext_vector_type(4)));
typedef unsigned u32x4 __attribute__((ext_vector_type(4)));
typedef unsigned u32x2 __attribute__((ext_vector_type(2)));
#define LAS __attribute__((address_space(3)))

constexpr int L_ = 2048, D_ = 576, NM_ = 128, F_ = 4608;
constexpr int HT_LD = 2048, U_LD = 4608;

__device__ __forceinline__ float bf2f(bf16_t v) { return __uint_as_float(((unsigned)v) << 16); }
__device__ __forceinline__ bf16_t f2bf(float f) {
  __hip_bfloat16 h = __float2bfloat16(f);
  return __builtin_bit_cast(unsigned short, h);
}
__device__ __forceinline__ float rbf(float f) { return bf2f(f2bf(f)); }
__device__ __forceinline__ unsigned pack2(float lo, float hi) { unsigned r; asm("v_cvt_pk_bf16_f32 %0, %1, %2" : "=v"(r) : "v"(lo), "v"(hi)); return r; }
typedef float f32x2 __attribute__((ext_vector_type(2)));

constexpr size_t WS_HT = 0;
constexpr size_t WS_AT = WS_HT + (size_t)D_ * HT_LD * 2;
constexpr size_t WS_G  = WS_AT + (size_t)D_ * L_ * 2;
constexpr size_t WS_U  = WS_G + (size_t)L_ * 4;
constexpr size_t WS_CNT = WS_U + (size_t)D_ * U_LD * 2;
constexpr size_t WS_P  = WS_CNT + 64 * 64 * 4;

constexpr int TB_STR = 528;
constexpr int SS_UL = 0;
constexpr int SS_KN = SS_UL + 4224;
constexpr int SS_XH = SS_KN + 2624;
constexpr int SS_XL = SS_XH + 41 * TB_STR;
constexpr int SS_YH = SS_XL + 41 * TB_STR;
constexpr int SS_YL = SS_YH + 16 * TB_STR;
constexpr int SS_TEND = SS_YL + 16 * TB_STR + 7 * TB_STR;
constexpr int SS_KH = SS_XH;
constexpr int SS_KL = SS_KH + 4608;
constexpr int SS_V = SS_KL + 4608;
constexpr int SS_VSTR = 4160;
constexpr int SS_END = SS_TEND;
static_assert(SS_V + 8 * SS_VSTR <= SS_END, "alias");
constexpr int SS_LN = 2 * SS_END;
constexpr int LN_STRIDE = 1152 + 16;
constexpr int SS_PF = SS_LN + 8 * LN_STRIDE;
constexpr int K1_LDS = SS_PF + 2048;

typedef float f32x2v __attribute__((ext_vector_type(2)));
__device__ __forceinline__ int prow_of(int r) {
  if (r < 16) return 128 * (r >> 3) + (r & 7);
  if (r < 24) return 256 + 128 * ((r - 16) >> 2) + 2 * ((r - 16) & 3);
  if (r < 32) return 512 + 128 * ((r - 24) >> 1) + 4 * ((r - 24) & 1);
  if (r <= 40) return 1024 + 128 * (r - 32);
  return 0;
}
__device__ __forceinline__ int pos_rank(int pos) {
  const int b = (int)(__float_as_uint((float)pos) >> 16) - 0x4380 + 256;
  return pos < 256 ? pos : b;
}
__device__ __forceinline__ f32x2 cexp_p(float are2, float aih, float ail, float p) {
  const float mag = __builtin_amdgcn_exp2f(are2 * p);
  const float pr = aih * p;
  const float er2 = __builtin_fmaf(aih, p, -pr);
  const float rr = __builtin_amdgcn_fractf(pr) + __builtin_fmaf(ail, p, er2);
  return (f32x2){__builtin_amdgcn_cosf(rr), __builtin_amdgcn_sinf(rr)} * mag;
}
__device__ __forceinline__ f32x2 cmul(f32x2 a, f32x2 b) {
  const f32x2 t = (f32x2){a[0], a[0]} * b;
  return __builtin_elementwise_fma((f32x2){a[1], a[1]}, (f32x2){-b[1], b[0]}, t);
}
__device__ __forceinline__ void split2(float a, float b, unsigned& hi, unsigned& lo) {
  hi = pack2(a, b);
  const f32x2 r = (f32x2){a, b} - (f32x2){__uint_as_float(hi << 16), __uint_as_float(hi & 0xffff0000u)};
  lo = pack2(r[0], r[1]);
}
__device__ __forceinline__ float wave_sum(float v) {
  v += __builtin_bit_cast(float, __builtin_amdgcn_update_dpp(0, __builtin_bit_cast(int, v), 0xB1, 0xF, 0xF, true));
  v += __builtin_bit_cast(float, __builtin_amdgcn_update_dpp(0, __builtin_bit_cast(int, v), 0x4E, 0xF, 0xF, true));
  v += __builtin_bit_cast(float, __builtin_amdgcn_update_dpp(0, __builtin_bit_cast(int, v), 0x141, 0xF, 0xF, true));
  v += __builtin_bit_cast(float, __builtin_amdgcn_update_dpp(0, __builtin_bit_cast(int, v), 0x140, 0xF, 0xF, true));
  const float r0 = __builtin_bit_cast(float, __builtin_amdgcn_readlane(__builtin_bit_cast(int, v), 0));
  const float r1 = __builtin_bit_cast(float, __builtin_amdgcn_readlane(__builtin_bit_cast(int, v), 16));
  const float r2 = __builtin_bit_cast(float, __builtin_amdgcn_readlane(__builtin_bit_cast(int, v), 32));
  const float r3 = __builtin_bit_cast(float, __builtin_amdgcn_readlane(__builtin_bit_cast(int, v), 48));
  return (r0 + r1) + (r2 + r3);
}

constexpr size_t SLAB_BYTES = (size_t)64 * 4 * 4 * 8 * 3 * 1024;
struct SsmArgs {
  const bf16_t* x; const bf16_t* logA; const bf16_t* Aim; const bf16_t* Cre; const bf16_t* Cim; const bf16_t* logdt; const bf16_t* Dskip;
  const bf16_t* gate_w; bf16_t* hT; bf16_t* aT; float* gate; const bf16_t* pf;
  unsigned* cnt; const bf16_t* pf2;
};

template <int ROWBYTES, int ROWS>
__device__ __forceinline__ void prefetch_rows(const char* base, size_t pitch, LAS char* scratch, int tid) {
  constexpr int SEG = ROWBYTES / 64, RPP = 512 / SEG;
  const int r0 = tid / SEG, sgm = tid - r0 * SEG;
  if (r0 < RPP) {
    const char* p = base + (size_t)r0 * pitch + sgm * 64;
#pragma unroll
    for (int r = 0; r < ROWS; r += RPP)
      if (r + r0 < ROWS) __builtin_amdgcn_global_load_lds((const unsigned*)(p + (size_t)r * pitch), (LAS unsigned*)(scratch + (tid >> 6) * 256), 4, 0, 0);
  }
}

#define LDS_BARRIER() do { asm volatile("" ::: "memory"); __builtin_amdgcn_s_waitcnt(0xC07F); __builtin_amdgcn_s_barrier(); asm volatile("" ::: "memory"); } while (0)
template <bool DO_LN>
__device__ __forceinline__ void ssm_wg(const SsmArgs& a, int d0, int lnrow0, const char* pfbase, size_t pfpitch, LAS char* smem_all) {
  const int tid = threadIdx.x, wid8 = __builtin_amdgcn_readfirstlane(tid >> 6), lane = tid & 63;
  const int half = wid8 >> 2, wid = wid8 & 3, t256 = tid & 255;
  const int d = d0 + half;
  LAS char* smem = smem_all + half * SS_END;
  const int mode = 64 * (wid & 1) + lane;
  const int mi = d * NM_ + mode;
  const bf16_t r_dt = a.logdt[d], r_ds = a.Dskip[d], r_la = a.logA[mi], r_ai = a.Aim[mi], r_cr = a.Cre[mi], r_ci = a.Cim[mi];
  __builtin_amdgcn_sched_barrier(0);
  u32x2 lnv[3], lnw[3];
  if (DO_LN) {
    const bf16_t* xr = a.x + (size_t)(lnrow0 + wid8) * D_;
#pragma unroll
    for (int k = 0; k < 3; ++k) {
      const int ch = (k < 2 || lane < 16) ? lane + 64 * k : 0;
      lnv[k] = *(const u32x2*)(xr + ch * 4);
      lnw[k] = *(const u32x2*)(a.gate_w + ch * 4);
    }
  }
  unsigned ucol[4];
#pragma unroll
  for (int k = 0; k < 4; ++k) ucol[k] = *(const unsigned*)(a.x + (size_t)(tid + 512 * k) * D_ + d0);
  __builtin_amdgcn_sched_barrier(0);
  const float dt = rbf(__expf(bf2f(r_dt)));
  const float dsk = bf2f(r_ds);
  {
    const float are2 = -rbf(__expf(bf2f(r_la))) * dt * 1.44269504088896340736f;
    const float aim = bf2f(r_ai) * dt;
    const float I2H = 0.159154936671257019043f;
    const float I2L = 6.42063824376987e-09f;
    const float aih = aim * I2H;
    const float ail = __builtin_fmaf(aim, I2H, -aih) + aim * I2L;
    LAS char* xh = smem + SS_XH + mode * 4;
    LAS char* xl = smem + SS_XL + mode * 4;
    auto put = [&](int row, f32x2 v) {
      unsigned h_, l_; split2(v[0], v[1], h_, l_);
      *(LAS unsigned*)(xh + row * TB_STR) = h_;
      *(LAS unsigned*)(xl + row * TB_STR) = l_;
    };
    const f32x2 z128 = cexp_p(are2, aih, ail, 128.f);
    if (wid < 2) {
      const f32x2 z1 = cexp_p(are2, aih, ail, 1.f);
      const f32x2 z8 = cexp_p(are2, aih, ail, 8.f);
      f32x2 e = {1.f, 0.f};
#pragma unroll
      for (int k = 0; k < 8; ++k) {
        put(k, e);
        put(8 + k, k ? cmul(z128, e) : z128);
        if (k < 7) e = k ? cmul(e, z1) : z1;
      }
      f32x2 y = {2.0f * bf2f(r_cr) * dt, 2.0f * bf2f(r_ci) * dt};
#pragma unroll
      for (int j = 0; j < 16; ++j) {
        unsigned h_, l_; split2(y[0], -y[1], h_, l_);
        *(LAS unsigned*)(smem + SS_YH + mode * 4 + j * TB_STR) = h_;
        *(LAS unsigned*)(smem + SS_YL + mode * 4 + j * TB_STR) = l_;
        if (j < 15) y = cmul(y, z8);
      }
    } else {
      const f32x2 z2 = cexp_p(are2, aih, ail, 2.f);
      const f32x2 z4 = cmul(z2, z2), z6 = cmul(z4, z2);
      f32x2 w = cmul(z128, z128);
#pragma unroll
      for (int i = 2; i <= 16; ++i) {
        if (i < 4) { const int r = 16 + 4 * (i - 2); put(r, w); put(r + 1, cmul(w, z2)); put(r + 2, cmul(w, z4)); put(r + 3, cmul(w, z6)); }
        else if (i < 8) { const int r = 24 + 2 * (i - 4); put(r, w); put(r + 1, cmul(w, z4)); }
        else put(32 + (i - 8), w);
        if (i < 16) w = cmul(w, z128);
      }
    }
  }
  if (t256 < 16) *(LAS bf16_t*)(smem + SS_UL + t256 * 2) = 0;
  if (t256 < 48) *(LAS bf16_t*)(smem + SS_UL + (16 + 2048 + t256) * 2) = 0;
#pragma unroll
  for (int k = 0; k < 4; ++k) {
    *(LAS bf16_t*)(smem_all + SS_UL + (16 + tid + 512 * k) * 2) = (bf16_t)(ucol[k] & 0xffffu);
    *(LAS bf16_t*)(smem_all + SS_END + SS_UL + (16 + tid + 512 * k) * 2) = (bf16_t)(ucol[k] >> 16);
  }
  if (DO_LN) {
    float xv[12], sum = 0.f, gs = 0.f;
#pragma unroll
    for (int k = 0; k < 3; ++k) {
      const bool ok = (k < 2 || lane < 16);
#pragma unroll
      for (int j = 0; j < 2; ++j) {
        const float a0 = ok ? __uint_as_float(lnv[k][j] << 16) : 0.f, a1 = ok ? __uint_as_float(lnv[k][j] & 0xffff0000u) : 0.f;
        const float w0 = __uint_as_float(lnw[k][j] << 16), w1 = __uint_as_float(lnw[k][j] & 0xffff0000u);
        xv[4 * k + 2 * j] = a0; xv[4 * k + 2 * j + 1] = a1;
        sum += a0 + a1;
        gs += rbf(a0 * w0) + rbf(a1 * w1);
      }
    }
    sum = wave_sum(sum); gs = wave_sum(gs);
    const float mean = sum * (1.0f / D_);
    float vs = 0.f;
#pragma unroll
    for (int k = 0; k < 3; ++k)
#pragma unroll
      for (int j = 0; j < 4; ++j) { const float dd = xv[4 * k + j] - mean; vs += (k < 2 || lane < 16) ? dd * dd : 0.f; }
    vs = wave_sum(vs);
    const float mb = rbf(mean);
    const float vb = rbf(vs * (1.0f / D_));
    const float den = rbf(sqrtf(rbf(vb + 1e-5f)));
    const float rden = 1.0f / den;
    if (lane == 0) a.gate[lnrow0 + wid8] = rbf(1.0f / (1.0f + expf(-rbf(gs))));
#pragma unroll
    for (int k = 0; k < 3; ++k)
      if (k < 2 || lane < 16) {
        u32x2 o = {pack2(rbf(xv[4 * k] - mb) * rden, rbf(xv[4 * k + 1] - mb) * rden), pack2(rbf(xv[4 * k + 2] - mb) * rden, rbf(xv[4 * k + 3] - mb) * rden)};
        *(LAS u32x2*)(smem_all + SS_LN + wid8 * LN_STRIDE + (lane + 64 * k) * 8) = o;
      }
  }
  if (DO_LN && blockIdx.x < 64 && tid == 0) a.cnt[blockIdx.x * 64] = 0u;
  if (DO_LN) {
    prefetch_rows<1152, 64>(pfbase, pfpitch, smem_all + SS_PF, tid);
    prefetch_rows<512, 144>((const char*)a.pf2 + (size_t)(blockIdx.x >> 3) * 144 * (L_ * 2) + (blockIdx.x & 7) * 512, (size_t)L_ * 2, smem_all + SS_PF, tid);
  }
  LDS_BARRIER();
  if (wid < 3) {
    const int i = lane & 15, g = lane >> 4;
    f32x4 acc = {0, 0, 0, 0};
    const LAS char* xa = smem + (16 * wid + i) * TB_STR + g * 16;
    const LAS char* ya = smem + i * TB_STR + g * 16;
#pragma unroll
    for (int t = 0; t < 8; ++t) {
      const bf16x8 XH = *(const LAS bf16x8*)(xa + SS_XH + t * 64), XL = *(const LAS bf16x8*)(xa + SS_XL + t * 64);
      const bf16x8 YH = *(const LAS bf16x8*)(ya + SS_YH + t * 64), YL = *(const LAS bf16x8*)(ya + SS_YL + t * 64);
      acc = __builtin_amdgcn_mfma_f32_16x16x32_bf16(XL, YH, acc, 0, 0, 0);
      acc = __builtin_amdgcn_mfma_f32_16x16x32_bf16(XH, YL, acc, 0, 0, 0);
      acc = __builtin_amdgcn_mfma_f32_16x16x32_bf16(XH, YH, acc, 0, 0, 0);
    }
#pragma unroll
    for (int j = 0; j < 4; ++j) {
      const int row = 16 * wid + 4 * g + j;
      if (row < 40 || (row == 40 && i == 0)) *(LAS float*)(smem + SS_KN + pos_rank(prow_of(row) + 8 * i) * 4) = acc[j];
    }
  } else if (DO_LN) {
    const int t128 = (half * 64 + lane);
#pragma unroll
    for (int k = 0; k < 5; ++k) {
      const int dd = t128 + 128 * k;
      if (dd < D_) {
        unsigned v[8];
#pragma unroll
        for (int r = 0; r < 8; ++r) v[r] = *(const LAS bf16_t*)(smem_all + SS_LN + r * LN_STRIDE + dd * 2);
        u32x4 o = {v[0] | (v[1] << 16), v[2] | (v[3] << 16), v[4] | (v[5] << 16), v[6] | (v[7] << 16)};
        *(u32x4*)(a.hT + (size_t)dd * HT_LD + lnrow0) = o;
      }
    }
  }
  LDS_BARRIER();
  {
    u32x4 oh, ol;
#pragma unroll
    for (int e = 0; e < 8; e += 2) {
      const int i0 = 2047 - (t256 * 8 + e), i1 = i0 - 1;
      const unsigned pb = pack2((float)i0, (float)i1);
      const int k0 = i0 < 256 ? i0 : (int)(pb & 0xffffu) - 0x4380 + 256;
      const int k1 = i1 < 256 ? i1 : (int)(pb >> 16) - 0x4380 + 256;
      const float v0 = *(const LAS float*)(smem + SS_KN + k0 * 4), v1 = *(const LAS float*)(smem + SS_KN + k1 * 4);
      unsigned h_, l_; split2(v0, v1, h_, l_);
      oh[e >> 1] = h_; ol[e >> 1] = l_;
    }
    *(LAS u32x4*)(smem + SS_KH + t256 * 16) = oh;
    *(LAS u32x4*)(smem + SS_KL + t256 * 16) = ol;
    if (t256 < 32) {
      *(LAS u32x4*)(smem + SS_KH + (256 + t256) * 16) = (u32x4){0, 0, 0, 0};
      *(LAS u32x4*)(smem + SS_KL + (256 + t256) * 16) = (u32x4){0, 0, 0, 0};
    }
  }
  {
    const u32x4 lo = *(const LAS u32x4*)(smem + SS_UL + t256 * 16);
    const u32x4 hi = *(const LAS u32x4*)(smem + SS_UL + t256 * 16 + 16);
    unsigned w[8] = {lo[0], lo[1], lo[2], lo[3], hi[0], hi[1], hi[2], hi[3]};
#pragma unroll
    for (int rho = 0; rho < 8; ++rho) {
      u32x4 o;
#pragma unroll
      for (int j = 0; j < 4; ++j) {
        const int b = (rho >> 1) + j;
        o[j] = (rho & 1) ? __builtin_amdgcn_alignbit(w[b + 1], w[b], 16) : w[b];
      }
      *(LAS u32x4*)(smem + SS_V + rho * SS_VSTR + t256 * 16) = o;
    }
    if (t256 < 32) {
      const int ch = 256 + (t256 & 3), rho = t256 >> 2;
      const LAS char* src = smem + SS_UL + ch * 16 + (rho >> 1) * 4;
      unsigned q[5];
#pragma unroll
      for (int j = 0; j < 5; ++j) q[j] = *(const LAS unsigned*)(src + j * 4);
      u32x4 o;
#pragma unroll
      for (int j = 0; j < 4; ++j) o[j] = (rho & 1) ? __builtin_amdgcn_alignbit(q[j + 1], q[j], 16) : q[j];
      *(LAS u32x4*)(smem + SS_V + rho * SS_VSTR + ch * 16) = o;
    }
  }
  LDS_BARRIER();
  f32x4 acc[8];
#pragma unroll
  for (int r = 0; r < 8; ++r) acc[r] = (f32x4){0, 0, 0, 0};
  {
    const int ij = lane & 15, g = lane >> 4;
    const int rho = (ij + 1) & 7, h = (ij + 1) >> 3;
    const LAS char* gbase = smem + SS_V + rho * SS_VSTR + (8 * g + 8 * h) * 2;
    const int kofs = (2032 - 16 * ij + 8 * g) * 2;
#pragma unroll 1
    for (int cc = 0; cc < 2; ++cc) {
      const int c = 2 * wid + cc;
      bf16x8 G[8];
#pragma unroll
      for (int b = 0; b < 8; ++b) G[b] = *(const LAS bf16x8*)(gbase + (256 * b + 32 * c) * 2);
#pragma unroll
      for (int aa = 0; aa < 8; ++aa) {
        const bf16x8 fh = *(const LAS bf16x8*)(smem + SS_KH + kofs + (32 * c - 256 * aa) * 2);
        const bf16x8 fl = *(const LAS bf16x8*)(smem + SS_KL + kofs + (32 * c - 256 * aa) * 2);
#pragma unroll
        for (int b = 0; b + aa < 8; ++b) {
          acc[aa + b] = __builtin_amdgcn_mfma_f32_16x16x32_bf16(G[b], fh, acc[aa + b], 0, 0, 0);
          acc[aa + b] = __builtin_amdgcn_mfma_f32_16x16x32_bf16(G[b], fl, acc[aa + b], 0, 0, 0);
        }
      }
    }
  }
  LDS_BARRIER();
#pragma unroll
  for (int r = 0; r < 8; ++r) *(LAS f32x4*)(smem + SS_V + (wid * 8 + r) * 1024 + lane * 16) = acc[r];
  LDS_BARRIER();
#pragma unroll
  for (int rr = 0; rr < 2; ++rr) {
    const int r = 2 * wid + rr;
    f32x4 s = *(const LAS f32x4*)(smem + SS_V + r * 1024 + lane * 16);
#pragma unroll
    for (int w = 1; w < 4; ++w) s += *(const LAS f32x4*)(smem + SS_V + (w * 8 + r) * 1024 + lane * 16);
    const int l0 = 256 * r + 16 * (lane & 15) + 4 * (lane >> 4);
    const bf16x4 uu = *(const LAS bf16x4*)(smem + SS_UL + (16 + l0) * 2);
    float o[4];
#pragma unroll
    for (int j = 0; j < 4; ++j) o[j] = rbf(s[j]) + rbf(bf2f((bf16_t)uu[j]) * dsk);
    u32x2 ov = {pack2(o[0], o[1]), pack2(o[2], o[3])};
    *(u32x2*)(a.aT + (size_t)d * L_ + l0) = ov;
  }
  asm volatile("s_waitcnt vmcnt(0)" ::: "memory");
}

__global__ __launch_bounds__(512) void k1_prep(SsmArgs a) {
  extern __shared__ __attribute__((aligned(16))) char smem_[];
  const int b = blockIdx.x;
  const int rb = (b & 7) * 32 + (b >> 3);
  const char* pf = (const char*)a.pf + (size_t)(b >> 3) * 64 * (F_ * 2) + (b & 7) * 1152;
  ssm_wg<true>(a, 2 * b, 8 * rb, pf, (size_t)F_ * 2, (LAS char*)smem_);
}

template <int AB> struct GemmCfg {
  static constexpr int ACT_BYTES = AB * 16 * 128;
  static constexpr int STAGE = ACT_BYTES + 64 * 256;
};

#define WAIT_VM(n) asm volatile("s_waitcnt vmcnt(%0)" ::"n"(n) : "memory")

template <int N> __device__ __forceinline__ void wait_vm() { asm volatile("s_waitcnt vmcnt(%0)" ::"n"(N) : "memory"); }

template <int AB, int ABW, int NG, int SW, int SA, int NT, bool ACT_A, int LDA, int LDW, int GRP>
__device__ __forceinline__ void gemm_wave(const bf16_t* __restrict__ actg,
                                          const bf16_t* __restrict__ wg,
                                          LAS char* smem, int w4, int lane, int ab0, f32x4 (&acc)[ABW][4]) {
  constexpr int ACTB = AB * 2048, WB = 16384, ABASE = SW * WB;
  constexpr int S = GRP == 0 ? SW : SA;
  constexpr int SMAX = SW > SA ? SW : SA;
  const int ww = w4 & 1;
  const char* gp;
  if (GRP == 0) {
    const int wf = (lane >> 4) | (((w4 >> 1) & 1) << 2);
    gp = (const char*)wg + (unsigned)(((lane >> 4) + 4 * w4) * LDW + (((lane & 15) ^ (wf << 1)) * 8)) * 2u;
  } else {
    gp = (const char*)actg + (unsigned)(((lane >> 3) + 8 * w4) * LDA + (((lane & 7) ^ (lane >> 3)) * 8)) * 2u;
  }
  auto issue = [&](int t, int buf) {
    LAS char* sb = smem + (GRP == 0 ? buf * WB : ABASE + buf * ACTB) + w4 * 1024;
    const char* g = gp + (size_t)t * (GRP == 0 ? (size_t)64 * LDW * 2 : (size_t)128);
#pragma unroll
    for (int j = 0; j < NG; ++j)
      __builtin_amdgcn_global_load_lds((const unsigned*)(g + (size_t)j * (GRP == 0 ? 16 * LDW * 2 : 32 * LDA * 2)), (LAS unsigned*)(sb + j * 4096), 16, 0, 0);
  };
  const int c = lane & 15, g = lane >> 4;
  const int ard = ABASE + (16 * ab0 + c) * 128 + (((4 * GRP + g) ^ (c & 7)) * 16);
  const int q4 = (lane & 15) >> 2, p4 = lane & 3;
  const int ff = q4 | ((g & 1) << 2);
  int wrd[4];
#pragma unroll
  for (int nb = 0; nb < 4; ++nb) wrd[nb] = GRP * 8192 + (8 * g + q4) * 256 + (((4 * ww + nb) ^ ff) * 32) + p4 * 8;

  bf16x8 fa0[ABW], fa1[ABW];
  bf16x4 fw0[8], fw1[8];
  auto step = [&](bf16x8 (&faM)[ABW], bf16x4 (&fwM)[8], bf16x8 (&faR)[ABW], bf16x4 (&fwR)[8], int bw, int ba, bool doRead, int tI, int bI, bool doIssue) {
    const LAS char* sw = smem + bw * WB;
    const LAS char* sa = smem + ba * ACTB;
    LAS char* sb = smem + (GRP == 0 ? bI * WB : ABASE + bI * ACTB) + w4 * 1024;
    const char* gsrc = gp + (size_t)tI * (GRP == 0 ? (size_t)64 * LDW * 2 : (size_t)128);
    constexpr int NR = 8 + ABW, NM = 4 * ABW;
#pragma unroll
    for (int z = 0; z < NM; ++z) {
      const int r0 = (NM >= NR + 4) ? z : (3 * z + 1) / 2, r1 = (NM >= NR + 4) ? z + 1 : (3 * (z + 1) + 1) / 2;
      if (doRead) {
#pragma unroll
        for (int r = r0; r < r1; ++r) {
          if (r < 8) fwR[r] = __builtin_amdgcn_ds_read_tr16_b64_v4i16((LAS bf16x4*)(sw + wrd[r >> 1] + (r & 1) * 1024));
          else if (r < NR) faR[r - 8] = *(const LAS bf16x8*)(sa + ard + (r - 8) * 2048);
        }
      }
      {
        const int ab = z >> 2, nb = z & 3;
        const bf16x8 wv = __builtin_shufflevector(fwM[2 * nb], fwM[2 * nb + 1], 0, 1, 2, 3, 4, 5, 6, 7);
        acc[ab][nb] = ACT_A ? __builtin_amdgcn_mfma_f32_16x16x32_bf16(faM[ab], wv, acc[ab][nb], 0, 0, 0)
                            : __builtin_amdgcn_mfma_f32_16x16x32_bf16(wv, faM[ab], acc[ab][nb], 0, 0, 0);
      }
      if (doIssue && z >= NM - NG - 1 && z < NM - 1) {
        const int j = z - (NM - NG - 1);
        __builtin_amdgcn_global_load_lds((const unsigned*)(gsrc + (size_t)j * (GRP == 0 ? 16 * LDW * 2 : 32 * LDA * 2)), (LAS unsigned*)(sb + j * 4096), 16, 0, 0);
      }
      __builtin_amdgcn_sched_barrier(0);
    }
  };
#define BARRIER() do { asm volatile("" ::: "memory"); __builtin_amdgcn_s_waitcnt(0xC07F); __builtin_amdgcn_s_barrier(); asm volatile("" ::: "memory"); } while (0)
#define SCHED() __builtin_amdgcn_sched_barrier(0)
#pragma unroll
  for (int z = 0; z < 8; ++z) fw1[z] = (bf16x4){0, 0, 0, 0};
#pragma unroll
  for (int ab = 0; ab < ABW; ++ab) fa1[ab] = (bf16x8){0, 0, 0, 0, 0, 0, 0, 0};
#pragma unroll
  for (int s = 0; s < S - 1; ++s) issue(s, s);
  int bw = 0, ba = 0, ibuf = S - 1;
  auto adv = [&]() {
    ibuf = (ibuf + 1 == S) ? 0 : ibuf + 1;
    bw = (bw + 1 == SW) ? 0 : bw + 1;
    ba = (ba + 1 == SA) ? 0 : ba + 1;
  };
  constexpr int NMAIN = ((NT - (SMAX - 1)) / 2) * 2;
#pragma unroll 1
  for (int t = 0; t < NMAIN; t += 2) {
    wait_vm<(S - 2) * NG>();
    BARRIER();
    SCHED();
    step(fa1, fw1, fa0, fw0, bw, ba, true, t + S - 1, ibuf, true);
    adv();
    wait_vm<(S - 2) * NG>();
    BARRIER();
    SCHED();
    step(fa0, fw0, fa1, fw1, bw, ba, true, t + S, ibuf, true);
    adv();
  }
#pragma unroll
  for (int t = NMAIN; t < NT; ++t) {
    switch ((t + S - 2 < NT - 1 ? S - 2 : NT - 1 - t)) {
      case 0: wait_vm<0>(); break;
      case 1: wait_vm<1 * NG>(); break;
      case 2: wait_vm<2 * NG>(); break;
      default: wait_vm<3 * NG>(); break;
    }
    BARRIER();
    SCHED();
    if ((t & 1) == 0) step(fa1, fw1, fa0, fw0, bw, ba, true, t + S - 1, ibuf, t + S - 1 < NT);
    else step(fa0, fw0, fa1, fw1, bw, ba, true, t + S - 1, ibuf, t + S - 1 < NT);
    adv();
  }
  __builtin_amdgcn_s_waitcnt(0xC07F);
  if ((NT & 1) == 0) step(fa1, fw1, fa0, fw0, 0, 0, false, 0, 0, false); else step(fa0, fw0, fa1, fw1, 0, 0, false, 0, 0, false);
}

__device__ __forceinline__ float gelu_tanh(float x) {
  const float c1 = -2.0f * 0.7978845608028654f * 1.4426950408889634f;
  const float c2 = c1 * 0.044715f;
  const float e = __builtin_amdgcn_exp2f(x * __builtin_fmaf(c2, x * x, c1));
  return x * __builtin_amdgcn_rcpf(1.0f + e);
}

template <int ABW, int GRP, int RSTR>
__device__ __forceinline__ void pair_reduce(f32x4 (&acc)[ABW][4], LAS char* smem, int w4, int lane) {
  __syncthreads();
  LAS char* mine = smem + (GRP * 4 + w4) * RSTR + lane * 16;
  const LAS char* theirs = smem + ((1 - GRP) * 4 + w4) * RSTR + lane * 16;
#pragma unroll
  for (int ab = 0; ab < ABW; ++ab)
#pragma unroll
    for (int h = 0; h < 2; ++h) *(LAS f32x4*)(mine + (ab * 2 + h) * 1024) = acc[ab][(GRP ? 0 : 2) + h];
  __syncthreads();
#pragma unroll
  for (int ab = 0; ab < ABW; ++ab)
#pragma unroll
    for (int h = 0; h < 2; ++h) acc[ab][(GRP ? 2 : 0) + h] += *(const LAS f32x4*)(theirs + (ab * 2 + h) * 1024);
}

#define G1SW 4
#define G1SA 4
constexpr int G1_LDS = G1SW * 16384 + G1SA * 6 * 2048;
template <int GRP>
__device__ __forceinline__ void g1_body(const bf16_t* __restrict__ hT, const bf16_t* __restrict__ w1, const bf16_t* __restrict__ b1,
                                        bf16_t* __restrict__ u, LAS char* smem, int w4, int lane, int d0, int n0) {
  f32x4 acc[3][4];
#pragma unroll
  for (int a = 0; a < 3; ++a)
#pragma unroll
    for (int n = 0; n < 4; ++n) acc[a][n] = (f32x4){0, 0, 0, 0};
  const int wa = w4 >> 1, ww = w4 & 1;
  gemm_wave<6, 3, GRP == 0 ? 4 : 3, G1SW, G1SA, 32, false, HT_LD, F_, GRP>(hT + (size_t)d0 * HT_LD, w1 + n0, smem, w4, lane, 3 * wa, acc);
  pair_reduce<3, GRP, 6144>(acc, smem, w4, lane);
  const int fq = lane >> 4, c = lane & 15;
#pragma unroll
  for (int h = 0; h < 2; ++h) {
    const int nb = 2 * GRP + h;
    const int f = n0 + 64 * ww + 16 * nb + 4 * fq;
    const bf16x4 bv = *(const bf16x4*)(b1 + f);
#pragma unroll
    for (int ab = 0; ab < 3; ++ab) {
      const int d = d0 + 48 * wa + 16 * ab + c;
      float o[4];
#pragma unroll
      for (int j = 0; j < 4; ++j) o[j] = gelu_tanh(rbf(rbf(acc[ab][nb][j]) + bf2f((bf16_t)bv[j])));
      u32x2 ov = {pack2(o[0], o[1]), pack2(o[2], o[3])};
      *(u32x2*)(u + (size_t)d * U_LD + f) = ov;
    }
  }
}
constexpr int K2_LDS = G1_LDS > K1_LDS ? G1_LDS : K1_LDS;
__global__ __launch_bounds__(512) void k2_gemm1(const bf16_t* __restrict__ hT, const bf16_t* __restrict__ w1, const bf16_t* __restrict__ b1,
                                                bf16_t* __restrict__ u, SsmArgs a) {
  extern __shared__ __attribute__((aligned(16))) char smem_[];
  LAS char* smem = (LAS char*)smem_;
  const int wid = __builtin_amdgcn_readfirstlane(threadIdx.x >> 6), lane = threadIdx.x & 63;
  const int b = blockIdx.x;
  if (b >= 216) {
    const int j = b - 216;
    const char* pf = (const char*)a.pf + (size_t)(j >> 3) * 1152 * (L_ * 2) + (j & 7) * 512;
    ssm_wg<false>(a, 512 + 2 * j, 0, pf, (size_t)L_ * 2, smem);
    return;
  }
  const int id = (b & 7) * 27 + (b >> 3);
  const int nt = id / 6, mt = id % 6;
  const int d0 = mt * 96, n0 = nt * 128;
  if (wid < 4) g1_body<0>(hT, w1, b1, u, smem, wid, lane, d0, n0);
  else g1_body<1>(hT, w1, b1, u, smem, wid - 4, lane, d0, n0);
}

#define G2SW 4
#define G2SA 4
constexpr int G2_LDS = G2SW * 16384 + G2SA * 9 * 2048;
struct G2Args {
  const bf16_t* u; const bf16_t* w2; float* slab; unsigned* cnt; const bf16_t* b2; const float* gate; const bf16_t* aT; bf16_t* out;
};
template <int ABW, int NG, int GRP>
__device__ __forceinline__ void g2_body(const G2Args& a, LAS char* smem, int w4, int lane, int d0, int s0, int ks, int ab0, int tile) {
  f32x4 acc[ABW][4];
#pragma unroll
  for (int x = 0; x < ABW; ++x)
#pragma unroll
    for (int n = 0; n < 4; ++n) acc[x][n] = (f32x4){0, 0, 0, 0};
  float gv[3]; bf16_t bv2r[3], avr[3][4];
  {
    const int fq = lane >> 4, c = lane & 15, ww = w4 & 1;
#pragma unroll
    for (int sl = 0; sl < 3; ++sl) {
      const int e = ks + 4 * sl;
      const int ee = e < 2 * ABW ? e : ks;
      const int s = s0 + 64 * ww + 16 * (2 * GRP + (ee & 1)) + c;
      const int d = d0 + 16 * (ab0 + (ee >> 1)) + 4 * fq;
      gv[sl] = a.gate[s];
      bv2r[sl] = a.b2[s];
#pragma unroll
      for (int j = 0; j < 4; ++j) avr[sl][j] = a.aT[(size_t)(d + j) * L_ + s];
    }
  }
  __builtin_amdgcn_sched_barrier(0);
  gemm_wave<9, ABW, NG, G2SW, G2SA, 18, true, U_LD, L_, GRP>(a.u + (size_t)d0 * U_LD + ks * 1152, a.w2 + (size_t)ks * 1152 * L_ + s0, smem, w4, lane, ab0, acc);
  pair_reduce<ABW, GRP, 10240>(acc, smem, w4, lane);
  const int fq = lane >> 4, c = lane & 15, ww = w4 & 1, wid8 = GRP * 4 + w4;
  const __amdgpu_buffer_rsrc_t rs = __builtin_amdgcn_make_buffer_rsrc((void*)a.slab, 0, (int)SLAB_BYTES, 0x00020000);
  constexpr int NE = 2 * ABW;
#pragma unroll
  for (int e = 0; e < NE; ++e) {
    const int owner = e & 3;
    if (owner != ks) {
      const int off = ((((tile * 4 + owner) * 4 + ks) * 8 + wid8) * 3 + (e >> 2)) * 1024 + lane * 16;
      __builtin_amdgcn_raw_buffer_store_b128(__builtin_bit_cast(u32x4, acc[e >> 1][2 * GRP + (e & 1)]), rs, off, 0, 16);
    }
  }
  asm volatile("s_waitcnt vmcnt(0)" ::: "memory");
  __syncthreads();
  if (wid8 == 0) {
    if (lane == 0) __hip_atomic_fetch_add(a.cnt + tile * 64, 1u, __ATOMIC_RELAXED, __HIP_MEMORY_SCOPE_AGENT);
    unsigned spins = 0;
    while ((unsigned)__builtin_amdgcn_readfirstlane(__hip_atomic_load(a.cnt + tile * 64, __ATOMIC_RELAXED, __HIP_MEMORY_SCOPE_AGENT)) < 4u) {
      __builtin_amdgcn_s_sleep(1);
      if (++spins > (1u << 24)) break;
    }
  }
  asm volatile("" ::: "memory");
  __syncthreads();
  asm volatile("" ::: "memory");
  constexpr int NSL = (NE + 3) / 4;
  u32x4 rv[NSL][3];
#pragma unroll
  for (int sl = 0; sl < NSL; ++sl)
#pragma unroll
    for (int k = 1; k < 4; ++k) {
      const int src = (ks + k) & 3;
      const int slc = (ks + 4 * sl < NE) ? sl : 0;
      const int off = ((((tile * 4 + ks) * 4 + src) * 8 + wid8) * 3 + slc) * 1024 + lane * 16;
      rv[sl][k - 1] = __builtin_amdgcn_raw_buffer_load_b128(rs, off, 0, 16);
    }
#pragma unroll
  for (int sl = 0; sl < NSL; ++sl) {
    const int e0 = 4 * sl;
    if (ks + e0 < NE) {
      f32x4 v = {0, 0, 0, 0};
#pragma unroll
      for (int k = 0; k < 4; ++k) if (k == ks && e0 + k < NE) v = acc[(e0 + k) >> 1][2 * GRP + ((e0 + k) & 1)];
#pragma unroll
      for (int k = 0; k < 3; ++k) v += __builtin_bit_cast(f32x4, rv[sl][k]);
      const int e = ks + e0;
      const int s = s0 + 64 * ww + 16 * (2 * GRP + (e & 1)) + c;
      const int d = d0 + 16 * (ab0 + (e >> 1)) + 4 * fq;
      const float g = gv[sl], omg = rbf(1.0f - g);
      float o[4];
#pragma unroll
      for (int j = 0; j < 4; ++j) o[j] = rbf(g * bf2f(avr[sl][j])) + rbf(omg * rbf(rbf(v[j]) + bf2f(bv2r[sl])));
      u32x2 ov = {pack2(o[0], o[1]), pack2(o[2], o[3])};
      *(u32x2*)(a.out + (size_t)s * D_ + d) = ov;
    }
  }
}
__global__ __launch_bounds__(512) void k3_gemm2(G2Args a) {
  extern __shared__ __attribute__((aligned(16))) char smem_[];
  LAS char* smem = (LAS char*)smem_;
  const int wid = __builtin_amdgcn_readfirstlane(threadIdx.x >> 6), lane = threadIdx.x & 63;
  const int b = blockIdx.x;
  const int id = (b & 7) * 32 + (b >> 3);
  const int st = id >> 4, ks = (id >> 2) & 3, dtile = id & 3;
  const int d0 = dtile * 144, s0 = st * 128, tile = st * 4 + dtile;
  switch (wid) {
    case 0: case 1: g2_body<5, 4, 0>(a, smem, wid, lane, d0, s0, ks, 0, tile); break;
    case 2: case 3: g2_body<4, 4, 0>(a, smem, wid, lane, d0, s0, ks, 5, tile); break;
    case 4: case 5: g2_body<4, 4, 1>(a, smem, wid - 2, lane, d0, s0, ks, 5, tile); break;
    default: g2_body<5, 5, 1>(a, smem, wid - 6, lane, d0, s0, ks, 0, tile); break;
  }
}

#define RUNK(k)
extern "C" void kernel_launch(void* const* d_in, const int* in_sizes, int n_in, void* d_out, int out_size, void* d_ws, size_t ws_size,
                              hipStream_t stream) {
  const bf16_t* x = (const bf16_t*)d_in[0];
  const bf16_t* logA = (const bf16_t*)d_in[1];
  const bf16_t* Aim = (const bf16_t*)d_in[2];
  const bf16_t* Cre = (const bf16_t*)d_in[3];
  const bf16_t* Cim = (const bf16_t*)d_in[4];
  const bf16_t* logdt = (const bf16_t*)d_in[5];
  const bf16_t* Dskip = (const bf16_t*)d_in[6];
  const bf16_t* w1 = (const bf16_t*)d_in[8];
  const bf16_t* b1 = (const bf16_t*)d_in[9];
  const bf16_t* w2 = (const bf16_t*)d_in[10];
  const bf16_t* b2 = (const bf16_t*)d_in[11];
  const bf16_t* gate_w = (const bf16_t*)d_in[12];
  char* ws = (char*)d_ws;
  bf16_t* hT = (bf16_t*)(ws + WS_HT);
  bf16_t* aT = (bf16_t*)(ws + WS_AT);
  float* gate = (float*)(ws + WS_G);
  bf16_t* u = (bf16_t*)(ws + WS_U);
  float* slab = (float*)(ws + WS_P);
  unsigned* cnt = (unsigned*)(ws + WS_CNT);
  (void)hipFuncSetAttribute((const void*)k1_prep, hipFuncAttributeMaxDynamicSharedMemorySize, K1_LDS);
  (void)hipFuncSetAttribute((const void*)k2_gemm1, hipFuncAttributeMaxDynamicSharedMemorySize, K2_LDS);
  (void)hipFuncSetAttribute((const void*)k3_gemm2, hipFuncAttributeMaxDynamicSharedMemorySize, G2_LDS);
  SsmArgs a1 = {x, logA, Aim, Cre, Cim, logdt, Dskip, gate_w, hT, aT, gate, w1, cnt, w2};
  SsmArgs a2 = {x, logA, Aim, Cre, Cim, logdt, Dskip, gate_w, hT, aT, gate, w2, cnt, w2};
  RUNK(0) k1_prep<<<256, 512, K1_LDS, stream>>>(a1);
  RUNK(1) k2_gemm1<<<216 + 32, 512, K2_LDS, stream>>>(hT, w1, b1, u, a2);
  G2Args g2 = {u, w2, slab, cnt, b2, gate, aT, (bf16_t*)d_out};
  RUNK(2) k3_gemm2<<<256, 512, G2_LDS, stream>>>(g2);
}
```

```cpp
#include <hip/hip_runtime.h>
#include <hip/hip_bf16.h>
#include <stdint.h>

typedef unsigned short bf16_t;
typedef short bf16x8 __attribute__((ext_vector_type(8)));
typedef short bf16x4 __attribute__((ext_vector_type(4)));
typedef float f32x4 __attribute__((ext_vector_type(4)));
typedef unsigned u32x4 __attribute__((ext_vector_type(4)));
typedef unsigned u32x2 __attribute__((ext_vector_type(2)));
#define LAS __attribute__((address_space(3)))

constexpr int L_ = 2048, D_ = 576, NM_ = 128, F_ = 4608;
constexpr int HT_LD = 2048, U_LD = 4608;

__device__ __forceinline__ float bf2f(bf16_t v) { return __uint_as_float(((unsigned)v) << 16); }
__device__ __forceinline__ bf16_t f2bf(float f) {
  __hip_bfloat16 h = __float2bfloat16(f);
  return __builtin_bit_cast(unsigned short, h);
}
__device__ __forceinline__ float rbf(float f) { return bf2f(f2bf(f)); }
typedef float f32x2 __attribute__((ext_vector_type(2)));
typedef __bf16 bf16x2v __attribute__((ext_vector_type(2)));
__device__ __forceinline__ unsigned pack2(float lo, float hi) {
  return __builtin_bit_cast(unsigned, __builtin_convertvector((f32x2){lo, hi}, bf16x2v));
}

constexpr size_t WS_HT = 0;
constexpr size_t WS_AT = WS_HT + (size_t)D_ * HT_LD * 2;
constexpr size_t WS_G  = WS_AT + (size_t)D_ * L_ * 2;
constexpr size_t WS_U  = WS_G + (size_t)L_ * 4;
constexpr size_t WS_CNT = WS_U + (size_t)D_ * U_LD * 2;
constexpr size_t WS_P  = WS_CNT + 64 * 64 * 4;

constexpr int TB_STR = 528;
constexpr int SS_UL = 0;
constexpr int SS_KN = SS_UL + 4224;
constexpr int SS_XH = SS_KN + 2624;
constexpr int SS_XL = SS_XH + 41 * TB_STR;
constexpr int SS_YH = SS_XL + 41 * TB_STR;
constexpr int SS_YL = SS_YH + 16 * TB_STR;
constexpr int SS_TEND = SS_YL + 16 * TB_STR + 7 * TB_STR;
constexpr int SS_KH = SS_XH;
constexpr int SS_KL = SS_KH + 4608;
constexpr int SS_V = SS_KL + 4608;
constexpr int SS_VSTR = 4160;
constexpr int SS_END = SS_TEND;
static_assert(SS_V + 8 * SS_VSTR <= SS_END, "alias");
constexpr int SS_LN = 2 * SS_END;
constexpr int LN_STRIDE = 1152 + 16;
constexpr int SS_PF = SS_LN + 8 * LN_STRIDE;
constexpr int K1_LDS = SS_PF + 2048;

__device__ __forceinline__ int prow_of(int r) {
  if (r < 16) return 128 * (r >> 3) + (r & 7);
  if (r < 24) return 256 + 128 * ((r - 16) >> 2) + 2 * ((r - 16) & 3);
  if (r < 32) return 512 + 128 * ((r - 24) >> 1) + 4 * ((r - 24) & 1);
  if (r <= 40) return 1024 + 128 * (r - 32);
  return 0;
}
__device__ __forceinline__ int pos_rank(int pos) {
  const int b = (int)(__float_as_uint((float)pos) >> 16) - 0x4380 + 256;
  return pos < 256 ? pos : b;
}
__device__ __forceinline__ f32x2 cexp_p(float are2, float aih, float ail, float p) {
  const float mag = __builtin_amdgcn_exp2f(are2 * p);
  const float pr = aih * p;
  const float er2 = __builtin_fmaf(aih, p, -pr);
  const float rr = __builtin_amdgcn_fractf(pr) + __builtin_fmaf(ail, p, er2);
  return (f32x2){__builtin_amdgcn_cosf(rr), __builtin_amdgcn_sinf(rr)} * mag;
}
__device__ __forceinline__ f32x2 cmul(f32x2 a, f32x2 b) {
  const f32x2 t = (f32x2){a[0], a[0]} * b;
  return __builtin_elementwise_fma((f32x2){a[1], a[1]}, (f32x2){-b[1], b[0]}, t);
}
__device__ __forceinline__ void split2(float a, float b, unsigned& hi, unsigned& lo) {
  hi = pack2(a, b);
  const f32x2 r = (f32x2){a, b} - (f32x2){__uint_as_float(hi << 16), __uint_as_float(hi & 0xffff0000u)};
  lo = pack2(r[0], r[1]);
}
__device__ __forceinline__ float wave_sum(float v) {
  v += __builtin_bit_cast(float, __builtin_amdgcn_update_dpp(0, __builtin_bit_cast(int, v), 0xB1, 0xF, 0xF, true));
  v += __builtin_bit_cast(float, __builtin_amdgcn_update_dpp(0, __builtin_bit_cast(int, v), 0x4E, 0xF, 0xF, true));
  v += __builtin_bit_cast(float, __builtin_amdgcn_update_dpp(0, __builtin_bit_cast(int, v), 0x141, 0xF, 0xF, true));
  v += __builtin_bit_cast(float, __builtin_amdgcn_update_dpp(0, __builtin_bit_cast(int, v), 0x140, 0xF, 0xF, true));
  const float r0 = __builtin_bit_cast(float, __builtin_amdgcn_readlane(__builtin_bit_cast(int, v), 0));
  const float r1 = __builtin_bit_cast(float, __builtin_amdgcn_readlane(__builtin_bit_cast(int, v), 16));
  const float r2 = __builtin_bit_cast(float, __builtin_amdgcn_readlane(__builtin_bit_cast(int, v), 32));
  const float r3 = __builtin_bit_cast(float, __builtin_amdgcn_readlane(__builtin_bit_cast(int, v), 48));
  return (r0 + r1) + (r2 + r3);
}

constexpr size_t SLAB_BYTES = (size_t)64 * 4 * 4 * 8 * 3 * 1024;
struct SsmArgs {
  const bf16_t* x; const bf16_t* logA; const bf16_t* Aim; const bf16_t* Cre; const bf16_t* Cim; const bf16_t* logdt; const bf16_t* Dskip;
  const bf16_t* gate_w; bf16_t* hT; bf16_t* aT; float* gate; const bf16_t* pf;
  unsigned* cnt; const bf16_t* pf2;
};

template <int ROWBYTES, int ROWS, int NTHR>
__device__ __forceinline__ void prefetch_rows(const char* base, size_t pitch, LAS char* scratch, int tid) {
  constexpr int SEG = ROWBYTES / 64, RPP = NTHR / SEG;
  const int r0 = tid / SEG, sgm = tid - r0 * SEG;
  if (r0 < RPP) {
    const char* p = base + (size_t)r0 * pitch + sgm * 64;
#pragma unroll
    for (int r = 0; r < ROWS; r += RPP)
      if (r + r0 < ROWS) __builtin_amdgcn_global_load_lds((const unsigned*)(p + (size_t)r * pitch), (LAS unsigned*)(scratch + (tid >> 6) * 256), 4, 0, 0);
  }
}

#define LDS_BARRIER() do { asm volatile("" ::: "memory"); __builtin_amdgcn_s_waitcnt(0xC07F); __builtin_amdgcn_s_barrier(); asm volatile("" ::: "memory"); } while (0)
template <bool DO_LN>
__device__ __forceinline__ void ssm_wg(const SsmArgs& a, int d0, int lnrow0, const char* pfbase, size_t pfpitch, LAS char* smem_all) {
  const int tid = threadIdx.x, wid8 = __builtin_amdgcn_readfirstlane(tid >> 6), lane = tid & 63;
  const int half = wid8 >> 2, wid = wid8 & 3, t256 = tid & 255;
  const int d = d0 + half;
  LAS char* smem = smem_all + half * SS_END;
  const int mode = 64 * (wid & 1) + lane;
  const int mi = d * NM_ + mode;
  const bf16_t r_dt = a.logdt[d], r_ds = a.Dskip[d], r_la = a.logA[mi], r_ai = a.Aim[mi], r_cr = a.Cre[mi], r_ci = a.Cim[mi];
  __builtin_amdgcn_sched_barrier(0);
  u32x2 lnv[3], lnw[3];
  if (DO_LN) {
    const bf16_t* xr = a.x + (size_t)(lnrow0 + wid8) * D_;
#pragma unroll
    for (int k = 0; k < 3; ++k) {
      const int ch = (k < 2 || lane < 16) ? lane + 64 * k : 0;
      lnv[k] = *(const u32x2*)(xr + ch * 4);
      lnw[k] = *(const u32x2*)(a.gate_w + ch * 4);
    }
  }
  unsigned ucol[4];
#pragma unroll
  for (int k = 0; k < 4; ++k) ucol[k] = *(const unsigned*)(a.x + (size_t)(tid + 512 * k) * D_ + d0);
  __builtin_amdgcn_sched_barrier(0);
  const float dt = rbf(__expf(bf2f(r_dt)));
  const float dsk = bf2f(r_ds);
  {
    const float are2 = -rbf(__expf(bf2f(r_la))) * dt * 1.44269504088896340736f;
    const float aim = bf2f(r_ai) * dt;
    const float I2H = 0.159154936671257019043f;
    const float I2L = 6.42063824376987e-09f;
    const float aih = aim * I2H;
    const float ail = __builtin_fmaf(aim, I2H, -aih) + aim * I2L;
    LAS char* xh = smem + SS_XH + mode * 4;
    LAS char* xl = smem + SS_XL + mode * 4;
    auto put = [&](int row, f32x2 v) {
      unsigned h_, l_; split2(v[0], v[1], h_, l_);
      *(LAS unsigned*)(xh + row * TB_STR) = h_;
      *(LAS unsigned*)(xl + row * TB_STR) = l_;
    };
    const f32x2 z128 = cexp_p(are2, aih, ail, 128.f);
    if (wid < 2) {
      const f32x2 z1 = cexp_p(are2, aih, ail, 1.f);
      const f32x2 z8 = cexp_p(are2, aih, ail, 8.f);
      f32x2 e = {1.f, 0.f};
#pragma unroll
      for (int k = 0; k < 8; ++k) {
        put(k, e);
        put(8 + k, k ? cmul(z128, e) : z128);
        if (k < 7) e = k ? cmul(e, z1) : z1;
      }
      f32x2 y = {2.0f * bf2f(r_cr) * dt, 2.0f * bf2f(r_ci) * dt};
#pragma unroll
      for (int j = 0; j < 16; ++j) {
        unsigned h_, l_; split2(y[0], -y[1], h_, l_);
        *(LAS unsigned*)(smem + SS_YH + mode * 4 + j * TB_STR) = h_;
        *(LAS unsigned*)(smem + SS_YL + mode * 4 + j * TB_STR) = l_;
        if (j < 15) y = cmul(y, z8);
      }
    } else {
      const f32x2 z2 = cexp_p(are2, aih, ail, 2.f);
      const f32x2 z4 = cmul(z2, z2), z6 = cmul(z4, z2);
      f32x2 w = cmul(z128, z128);
#pragma unroll
      for (int i = 2; i <= 16; ++i) {
        if (i < 4) { const int r = 16 + 4 * (i - 2); put(r, w); put(r + 1, cmul(w, z2)); put(r + 2, cmul(w, z4)); put(r + 3, cmul(w, z6)); }
        else if (i < 8) { const int r = 24 + 2 * (i - 4); put(r, w); put(r + 1, cmul(w, z4)); }
        else put(32 + (i - 8), w);
        if (i < 16) w = cmul(w, z128);
      }
    }
  }
  if (t256 < 16) *(LAS bf16_t*)(smem + SS_UL + t256 * 2) = 0;
  if (t256 < 48) *(LAS bf16_t*)(smem + SS_UL + (16 + 2048 + t256) * 2) = 0;
#pragma unroll
  for (int k = 0; k < 4; ++k) {
    *(LAS bf16_t*)(smem_all + SS_UL + (16 + tid + 512 * k) * 2) = (bf16_t)(ucol[k] & 0xffffu);
    *(LAS bf16_t*)(smem_all + SS_END + SS_UL + (16 + tid + 512 * k) * 2) = (bf16_t)(ucol[k] >> 16);
  }
  if (DO_LN) {
    float xv[12], sum = 0.f, gs = 0.f;
#pragma unroll
    for (int k = 0; k < 3; ++k) {
      const bool ok = (k < 2 || lane < 16);
#pragma unroll
      for (int j = 0; j < 2; ++j) {
        const float a0 = ok ? __uint_as_float(lnv[k][j] << 16) : 0.f, a1 = ok ? __uint_as_float(lnv[k][j] & 0xffff0000u) : 0.f;
        const float w0 = __uint_as_float(lnw[k][j] << 16), w1 = __uint_as_float(lnw[k][j] & 0xffff0000u);
        xv[4 * k + 2 * j] = a0; xv[4 * k + 2 * j + 1] = a1;
        sum += a0 + a1;
        gs += rbf(a0 * w0) + rbf(a1 * w1);
      }
    }
    sum = wave_sum(sum); gs = wave_sum(gs);
    const float mean = sum * (1.0f / D_);
    float vs = 0.f;
#pragma unroll
    for (int k = 0; k < 3; ++k)
#pragma unroll
      for (int j = 0; j < 4; ++j) { const float dd = xv[4 * k + j] - mean; vs += (k < 2 || lane < 16) ? dd * dd : 0.f; }
    vs = wave_sum(vs);
    const float mb = rbf(mean);
    const float vb = rbf(vs * (1.0f / D_));
    const float den = rbf(sqrtf(rbf(vb + 1e-5f)));
    const float rden = 1.0f / den;
    if (lane == 0) a.gate[lnrow0 + wid8] = rbf(1.0f / (1.0f + expf(-rbf(gs))));
#pragma unroll
    for (int k = 0; k < 3; ++k)
      if (k < 2 || lane < 16) {
        u32x2 o = {pack2(rbf(xv[4 * k] - mb) * rden, rbf(xv[4 * k + 1] - mb) * rden), pack2(rbf(xv[4 * k + 2] - mb) * rden, rbf(xv[4 * k + 3] - mb) * rden)};
        *(LAS u32x2*)(smem_all + SS_LN + wid8 * LN_STRIDE + (lane + 64 * k) * 8) = o;
      }
  }
  if (DO_LN && blockIdx.x < 64 && tid == 0) a.cnt[blockIdx.x * 64] = 0u;
  LDS_BARRIER();
  if (wid < 3) {
    const int i = lane & 15, g = lane >> 4;
    f32x4 acc = {0, 0, 0, 0};
    const LAS char* xa = smem + (16 * wid + i) * TB_STR + g * 16;
    const LAS char* ya = smem + i * TB_STR + g * 16;
#pragma unroll
    for (int t = 0; t < 8; ++t) {
      const bf16x8 XH = *(const LAS bf16x8*)(xa + SS_XH + t * 64), XL = *(const LAS bf16x8*)(xa + SS_XL + t * 64);
      const bf16x8 YH = *(const LAS bf16x8*)(ya + SS_YH + t * 64), YL = *(const LAS bf16x8*)(ya + SS_YL + t * 64);
      acc = __builtin_amdgcn_mfma_f32_16x16x32_bf16(XL, YH, acc, 0, 0, 0);
      acc = __builtin_amdgcn_mfma_f32_16x16x32_bf16(XH, YL, acc, 0, 0, 0);
      acc = __builtin_amdgcn_mfma_f32_16x16x32_bf16(XH, YH, acc, 0, 0, 0);
    }
#pragma unroll
    for (int j = 0; j < 4; ++j) {
      const int row = 16 * wid + 4 * g + j;
      if (row < 40 || (row == 40 && i == 0)) *(LAS float*)(smem + SS_KN + pos_rank(prow_of(row) + 8 * i) * 4) = acc[j];
    }
  } else if (DO_LN) {
    const int t128 = (half * 64 + lane);
#pragma unroll
    for (int k = 0; k < 5; ++k) {
      const int dd = t128 + 128 * k;
      if (dd < D_) {
        unsigned v[8];
#pragma unroll
        for (int r = 0; r < 8; ++r) v[r] = *(const LAS bf16_t*)(smem_all + SS_LN + r * LN_STRIDE + dd * 2);
        u32x4 o = {v[0] | (v[1] << 16), v[2] | (v[3] << 16), v[4] | (v[5] << 16), v[6] | (v[7] << 16)};
        *(u32x4*)(a.hT + (size_t)dd * HT_LD + lnrow0) = o;
      }
    }
  }
  LDS_BARRIER();
  {
    u32x4 oh, ol;
#pragma unroll
    for (int e = 0; e < 8; e += 2) {
      const int i0 = 2047 - (t256 * 8 + e), i1 = i0 - 1;
      const unsigned pb = pack2((float)i0, (float)i1);
      const int k0 = i0 < 256 ? i0 : (int)(pb & 0xffffu) - 0x4380 + 256;
      const int k1 = i1 < 256 ? i1 : (int)(pb >> 16) - 0x4380 + 256;
      const float v0 = *(const LAS float*)(smem + SS_KN + k0 * 4), v1 = *(const LAS float*)(smem + SS_KN + k1 * 4);
      unsigned h_, l_; split2(v0, v1, h_, l_);
      oh[e >> 1] = h_; ol[e >> 1] = l_;
    }
    *(LAS u32x4*)(smem + SS_KH + t256 * 16) = oh;
    *(LAS u32x4*)(smem + SS_KL + t256 * 16) = ol;
    if (t256 < 32) {
      *(LAS u32x4*)(smem + SS_KH + (256 + t256) * 16) = (u32x4){0, 0, 0, 0};
      *(LAS u32x4*)(smem + SS_KL + (256 + t256) * 16) = (u32x4){0, 0, 0, 0};
    }
  }
  {
    const u32x4 lo = *(const LAS u32x4*)(smem + SS_UL + t256 * 16);
    const u32x4 hi = *(const LAS u32x4*)(smem + SS_UL + t256 * 16 + 16);
    unsigned w[8] = {lo[0], lo[1], lo[2], lo[3], hi[0], hi[1], hi[2], hi[3]};
#pragma unroll
    for (int rho = 0; rho < 8; ++rho) {
      u32x4 o;
#pragma unroll
      for (int j = 0; j < 4; ++j) {
        const int b = (rho >> 1) + j;
        o[j] = (rho & 1) ? __builtin_amdgcn_alignbit(w[b + 1], w[b], 16) : w[b];
      }
      *(LAS u32x4*)(smem + SS_V + rho * SS_VSTR + t256 * 16) = o;
    }
    if (t256 < 32) {
      const int ch = 256 + (t256 & 3), rho = t256 >> 2;
      const LAS char* src = smem + SS_UL + ch * 16 + (rho >> 1) * 4;
      unsigned q[5];
#pragma unroll
      for (int j = 0; j < 5; ++j) q[j] = *(const LAS unsigned*)(src + j * 4);
      u32x4 o;
#pragma unroll
      for (int j = 0; j < 4; ++j) o[j] = (rho & 1) ? __builtin_amdgcn_alignbit(q[j + 1], q[j], 16) : q[j];
      *(LAS u32x4*)(smem + SS_V + rho * SS_VSTR + ch * 16) = o;
    }
  }
  LDS_BARRIER();
  f32x4 acc[8];
#pragma unroll
  for (int r = 0; r < 8; ++r) acc[r] = (f32x4){0, 0, 0, 0};
  {
    const int ij = lane & 15, g = lane >> 4;
    const int rho = (ij + 1) & 7, h = (ij + 1) >> 3;
    const LAS char* gbase = smem + SS_V + rho * SS_VSTR + (8 * g + 8 * h) * 2;
    const int kofs = (2032 - 16 * ij + 8 * g) * 2;
#pragma unroll 1
    for (int cc = 0; cc < 2; ++cc) {
      const int c = 2 * wid + cc;
      bf16x8 G[8];
#pragma unroll
      for (int b = 0; b < 8; ++b) G[b] = *(const LAS bf16x8*)(gbase + (256 * b + 32 * c) * 2);
#pragma unroll
      for (int aa = 0; aa < 8; ++aa) {
        const bf16x8 fh = *(const LAS bf16x8*)(smem + SS_KH + kofs + (32 * c - 256 * aa) * 2);
        const bf16x8 fl = *(const LAS bf16x8*)(smem + SS_KL + kofs + (32 * c - 256 * aa) * 2);
#pragma unroll
        for (int b = 0; b + aa < 8; ++b) {
          acc[aa + b] = __builtin_amdgcn_mfma_f32_16x16x32_bf16(G[b], fh, acc[aa + b], 0, 0, 0);
          acc[aa + b] = __builtin_amdgcn_mfma_f32_16x16x32_bf16(G[b], fl, acc[aa + b], 0, 0, 0);
        }
      }
    }
  }
  LDS_BARRIER();
#pragma unroll
  for (int r = 0; r < 8; ++r) *(LAS f32x4*)(smem + SS_V + (wid * 8 + r) * 1024 + lane * 16) = acc[r];
  LDS_BARRIER();
#pragma unroll
  for (int rr = 0; rr < 2; ++rr) {
    const int r = 2 * wid + rr;
    f32x4 s = *(const LAS f32x4*)(smem + SS_V + r * 1024 + lane * 16);
#pragma unroll
    for (int w = 1; w < 4; ++w) s += *(const LAS f32x4*)(smem + SS_V + (w * 8 + r) * 1024 + lane * 16);
    const int l0 = 256 * r + 16 * (lane & 15) + 4 * (lane >> 4);
    const bf16x4 uu = *(const LAS bf16x4*)(smem + SS_UL + (16 + l0) * 2);
    float o[4];
#pragma unroll
    for (int j = 0; j < 4; ++j) o[j] = rbf(s[j]) + rbf(bf2f((bf16_t)uu[j]) * dsk);
    u32x2 ov = {pack2(o[0], o[1]), pack2(o[2], o[3])};
    *(u32x2*)(a.aT + (size_t)d * L_ + l0) = ov;
  }
  asm volatile("s_waitcnt vmcnt(0)" ::: "memory");
}

constexpr int SSM_WG_BARRIERS = 5;
__global__ __launch_bounds__(768) void k1_prep(SsmArgs a) {
  extern __shared__ __attribute__((aligned(16))) char smem_[];
  const int b = blockIdx.x;
  if (threadIdx.x >= 512) {
    const int t = threadIdx.x - 512;
    LAS char* scratch = (LAS char*)smem_ + SS_PF;
    prefetch_rows<1152, 64, 256>((const char*)a.pf + (size_t)(b >> 3) * 64 * (F_ * 2) + (b & 7) * 1152, (size_t)F_ * 2, scratch, t);
    prefetch_rows<512, 144, 256>((const char*)a.pf2 + (size_t)(b >> 3) * 144 * (L_ * 2) + (b & 7) * 512, (size_t)L_ * 2, scratch, t);
#pragma unroll
    for (int i = 0; i < SSM_WG_BARRIERS; ++i) __builtin_amdgcn_s_barrier();
    asm volatile("s_waitcnt vmcnt(0)" ::: "memory");
    return;
  }
  const int rb = (b & 7) * 32 + (b >> 3);
  ssm_wg<true>(a, 2 * b, 8 * rb, nullptr, 0, (LAS char*)smem_);
}

template <int AB> struct GemmCfg {
  static constexpr int ACT_BYTES = AB * 16 * 128;
  static constexpr int STAGE = ACT_BYTES + 64 * 256;
};

#define WAIT_VM(n) asm volatile("s_waitcnt vmcnt(%0)" ::"n"(n) : "memory")

template <int N> __device__ __forceinline__ void wait_vm() { asm volatile("s_waitcnt vmcnt(%0)" ::"n"(N) : "memory"); }

template <int AB, int ABW, int NG, int SW, int SA, int NT, bool ACT_A, int LDA, int LDW, int GRP>
__device__ __forceinline__ void gemm_wave(const bf16_t* __restrict__ actg,
                                          const bf16_t* __restrict__ wg,
                                          LAS char* smem, int w4, int lane, int ab0, f32x4 (&acc)[ABW][4]) {
  constexpr int ACTB = AB * 2048, WB = 16384, ABASE = SW * WB;
  constexpr int S = GRP == 0 ? SW : SA;
  constexpr int SMAX = SW > SA ? SW : SA;
  const int ww = w4 & 1;
  const char* gp;
  if (GRP == 0) {
    const int wf = (lane >> 4) | (((w4 >> 1) & 1) << 2);
    gp = (const char*)wg + (unsigned)(((lane >> 4) + 4 * w4) * LDW + (((lane & 15) ^ (wf << 1)) * 8)) * 2u;
  } else {
    gp = (const char*)actg + (unsigned)(((lane >> 3) + 8 * w4) * LDA + (((lane & 7) ^ (lane >> 3)) * 8)) * 2u;
  }
  auto issue = [&](int t, int buf) {
    LAS char* sb = smem + (GRP == 0 ? buf * WB : ABASE + buf * ACTB) + w4 * 1024;
    const char* g = gp + (size_t)t * (GRP == 0 ? (size_t)64 * LDW * 2 : (size_t)128);
#pragma unroll
    for (int j = 0; j < NG; ++j)
      __builtin_amdgcn_global_load_lds((const unsigned*)(g + (size_t)j * (GRP == 0 ? 16 * LDW * 2 : 32 * LDA * 2)), (LAS unsigned*)(sb + j * 4096), 16, 0, 0);
  };
  const int c = lane & 15, g = lane >> 4;
  const int ard = ABASE + (16 * ab0 + c) * 128 + (((4 * GRP + g) ^ (c & 7)) * 16);
  const int q4 = (lane & 15) >> 2, p4 = lane & 3;
  const int ff = q4 | ((g & 1) << 2);
  int wrd[4];
#pragma unroll
  for (int nb = 0; nb < 4; ++nb) wrd[nb] = GRP * 8192 + (8 * g + q4) * 256 + (((4 * ww + nb) ^ ff) * 32) + p4 * 8;

  bf16x8 fa0[ABW], fa1[ABW];
  bf16x4 fw0[8], fw1[8];
  auto step = [&](bf16x8 (&faM)[ABW], bf16x4 (&fwM)[8], bf16x8 (&faR)[ABW], bf16x4 (&fwR)[8], int bw, int ba, bool doRead, int tI, int bI, bool doIssue) {
    const LAS char* sw = smem + bw * WB;
    const LAS char* sa = smem + ba * ACTB;
    LAS char* sb = smem + (GRP == 0 ? bI * WB : ABASE + bI * ACTB) + w4 * 1024;
    const char* gsrc = gp + (size_t)tI * (GRP == 0 ? (size_t)64 * LDW * 2 : (size_t)128);
    constexpr int NR = 8 + ABW, NM = 4 * ABW;
#pragma unroll
    for (int z = 0; z < NM; ++z) {
      const int r0 = (NM >= NR + 4) ? z : (3 * z + 1) / 2, r1 = (NM >= NR + 4) ? z + 1 : (3 * (z + 1) + 1) / 2;
      if (doRead) {
#pragma unroll
        for (int r = r0; r < r1; ++r) {
          if (r < 8) fwR[r] = __builtin_amdgcn_ds_read_tr16_b64_v4i16((LAS bf16x4*)(sw + wrd[r >> 1] + (r & 1) * 1024));
          else if (r < NR) faR[r - 8] = *(const LAS bf16x8*)(sa + ard + (r - 8) * 2048);
        }
      }
      {
        const int ab = z >> 2, nb = z & 3;
        const bf16x8 wv = __builtin_shufflevector(fwM[2 * nb], fwM[2 * nb + 1], 0, 1, 2, 3, 4, 5, 6, 7);
        acc[ab][nb] = ACT_A ? __builtin_amdgcn_mfma_f32_16x16x32_bf16(faM[ab], wv, acc[ab][nb], 0, 0, 0)
                            : __builtin_amdgcn_mfma_f32_16x16x32_bf16(wv, faM[ab], acc[ab][nb], 0, 0, 0);
      }
      if (doIssue && z >= NM - NG - 1 && z < NM - 1) {
        const int j = z - (NM - NG - 1);
        __builtin_amdgcn_global_load_lds((const unsigned*)(gsrc + (size_t)j * (GRP == 0 ? 16 * LDW * 2 : 32 * LDA * 2)), (LAS unsigned*)(sb + j * 4096), 16, 0, 0);
      }
      __builtin_amdgcn_sched_barrier(0);
    }
  };
#define BARRIER() do { asm volatile("" ::: "memory"); __builtin_amdgcn_s_waitcnt(0xC07F); __builtin_amdgcn_s_barrier(); asm volatile("" ::: "memory"); } while (0)
#define SCHED() __builtin_amdgcn_sched_barrier(0)
#pragma unroll
  for (int z = 0; z < 8; ++z) fw1[z] = (bf16x4){0, 0, 0, 0};
#pragma unroll
  for (int ab = 0; ab < ABW; ++ab) fa1[ab] = (bf16x8){0, 0, 0, 0, 0, 0, 0, 0};
#pragma unroll
  for (int s = 0; s < S - 1; ++s) issue(s, s);
  int bw = 0, ba = 0, ibuf = S - 1;
  auto adv = [&]() {
    ibuf = (ibuf + 1 == S) ? 0 : ibuf + 1;
    bw = (bw + 1 == SW) ? 0 : bw + 1;
    ba = (ba + 1 == SA) ? 0 : ba + 1;
  };
  constexpr int NMAIN = ((NT - (SMAX - 1)) / 2) * 2;
#pragma unroll 1
  for (int t = 0; t < NMAIN; t += 2) {
    wait_vm<(S - 2) * NG>();
    BARRIER();
    SCHED();
    step(fa1, fw1, fa0, fw0, bw, ba, true, t + S - 1, ibuf, true);
    adv();
    wait_vm<(S - 2) * NG>();
    BARRIER();
    SCHED();
    step(fa0, fw0, fa1, fw1, bw, ba, true, t + S, ibuf, true);
    adv();
  }
#pragma unroll
  for (int t = NMAIN; t < NT; ++t) {
    switch ((t + S - 2 < NT - 1 ? S - 2 : NT - 1 - t)) {
      case 0: wait_vm<0>(); break;
      case 1: wait_vm<1 * NG>(); break;
      case 2: wait_vm<2 * NG>(); break;
      default: wait_vm<3 * NG>(); break;
    }
    BARRIER();
    SCHED();
    if ((t & 1) == 0) step(fa1, fw1, fa0, fw0, bw, ba, true, t + S - 1, ibuf, t + S - 1 < NT);
    else step(fa0, fw0, fa1, fw1, bw, ba, true, t + S - 1, ibuf, t + S - 1 < NT);
    adv();
  }
  __builtin_amdgcn_s_waitcnt(0xC07F);
  if ((NT & 1) == 0) step(fa1, fw1, fa0, fw0, 0, 0, false, 0, 0, false); else step(fa0, fw0, fa1, fw1, 0, 0, false, 0, 0, false);
}

__device__ __forceinline__ float gelu_tanh(float x) {
  const float c1 = -2.0f * 0.7978845608028654f * 1.4426950408889634f;
  const float c2 = c1 * 0.044715f;
  const float e = __builtin_amdgcn_exp2f(x * __builtin_fmaf(c2, x * x, c1));
  return x * __builtin_amdgcn_rcpf(1.0f + e);
}

template <int ABW, int GRP, int RSTR>
__device__ __forceinline__ void pair_reduce(f32x4 (&acc)[ABW][4], LAS char* smem, int w4, int lane) {
  __syncthreads();
  LAS char* mine = smem + (GRP * 4 + w4) * RSTR + lane * 16;
  const LAS char* theirs = smem + ((1 - GRP) * 4 + w4) * RSTR + lane * 16;
#pragma unroll
  for (int ab = 0; ab < ABW; ++ab)
#pragma unroll
    for (int h = 0; h < 2; ++h) *(LAS f32x4*)(mine + (ab * 2 + h) * 1024) = acc[ab][(GRP ? 0 : 2) + h];
  __syncthreads();
#pragma unroll
  for (int ab = 0; ab < ABW; ++ab)
#pragma unroll
    for (int h = 0; h < 2; ++h) acc[ab][(GRP ? 2 : 0) + h] += *(const LAS f32x4*)(theirs + (ab * 2 + h) * 1024);
}

#define G1SW 4
#define G1SA 4
constexpr int G1_LDS = G1SW * 16384 + G1SA * 6 * 2048;
template <int GRP>
__device__ __forceinline__ void g1_body(const bf16_t* __restrict__ hT, const bf16_t* __restrict__ w1, const bf16_t* __restrict__ b1,
                                        bf16_t* __restrict__ u, LAS char* smem, int w4, int lane, int d0, int n0) {
  f32x4 acc[3][4];
#pragma unroll
  for (int a = 0; a < 3; ++a)
#pragma unroll
    for (int n = 0; n < 4; ++n) acc[a][n] = (f32x4){0, 0, 0, 0};
  const int wa = w4 >> 1, ww = w4 & 1;
  gemm_wave<6, 3, GRP == 0 ? 4 : 3, G1SW, G1SA, 32, false, HT_LD, F_, GRP>(hT + (size_t)d0 * HT_LD, w1 + n0, smem, w4, lane, 3 * wa, acc);
  pair_reduce<3, GRP, 6144>(acc, smem, w4, lane);
  const int fq = lane >> 4, c = lane & 15;
#pragma unroll
  for (int h = 0; h < 2; ++h) {
    const int nb = 2 * GRP + h;
    const int f = n0 + 64 * ww + 16 * nb + 4 * fq;
    const bf16x4 bv = *(const bf16x4*)(b1 + f);
#pragma unroll
    for (int ab = 0; ab < 3; ++ab) {
      const int d = d0 + 48 * wa + 16 * ab + c;
      float o[4];
#pragma unroll
      for (int j = 0; j < 4; ++j) o[j] = gelu_tanh(rbf(rbf(acc[ab][nb][j]) + bf2f((bf16_t)bv[j])));
      u32x2 ov = {pack2(o[0], o[1]), pack2(o[2], o[3])};
      *(u32x2*)(u + (size_t)d * U_LD + f) = ov;
    }
  }
}
constexpr int K2_LDS = G1_LDS > K1_LDS ? G1_LDS : K1_LDS;
__global__ __launch_bounds__(512) void k2_gemm1(const bf16_t* __restrict__ hT, const bf16_t* __restrict__ w1, const bf16_t* __restrict__ b1,
                                                bf16_t* __restrict__ u, SsmArgs a) {
  extern __shared__ __attribute__((aligned(16))) char smem_[];
  LAS char* smem = (LAS char*)smem_;
  const int wid = __builtin_amdgcn_readfirstlane(threadIdx.x >> 6), lane = threadIdx.x & 63;
  const int b = blockIdx.x;
  if (b >= 216) {
    const int j = b - 216;
    const char* pf = (const char*)a.pf + (size_t)(j >> 3) * 1152 * (L_ * 2) + (j & 7) * 512;
    ssm_wg<false>(a, 512 + 2 * j, 0, pf, (size_t)L_ * 2, smem);
    return;
  }
  const int id = (b & 7) * 27 + (b >> 3);
  const int nt = id / 6, mt = id % 6;
  const int d0 = mt * 96, n0 = nt * 128;
  if (wid < 4) g1_body<0>(hT, w1, b1, u, smem, wid, lane, d0, n0);
  else g1_body<1>(hT, w1, b1, u, smem, wid - 4, lane, d0, n0);
}

#define G2SW 4
#define G2SA 4
constexpr int G2_LDS = G2SW * 16384 + G2SA * 9 * 2048;
struct G2Args {
  const bf16_t* u; const bf16_t* w2; float* slab; unsigned* cnt; const bf16_t* b2; const float* gate; const bf16_t* aT; bf16_t* out;
};
template <int ABW, int NG, int GRP>
__device__ __forceinline__ void g2_body(const G2Args& a, LAS char* smem, int w4, int lane, int d0, int s0, int ks, int ab0, int tile) {
  f32x4 acc[ABW][4];
#pragma unroll
  for (int x = 0; x < ABW; ++x)
#pragma unroll
    for (int n = 0; n < 4; ++n) acc[x][n] = (f32x4){0, 0, 0, 0};
  float gv[3]; bf16_t bv2r[3], avr[3][4];
  {
    const int fq = lane >> 4, c = lane & 15, ww = w4 & 1;
#pragma unroll
    for (int sl = 0; sl < 3; ++sl) {
      const int e = ks + 4 * sl;
      const int ee = e < 2 * ABW ? e : ks;
      const int s = s0 + 64 * ww + 16 * (2 * GRP + (ee & 1)) + c;
      const int d = d0 + 16 * (ab0 + (ee >> 1)) + 4 * fq;
      gv[sl] = a.gate[s];
      bv2r[sl] = a.b2[s];
#pragma unroll
      for (int j = 0; j < 4; ++j) avr[sl][j] = a.aT[(size_t)(d + j) * L_ + s];
    }
  }
  __builtin_amdgcn_sched_barrier(0);
  gemm_wave<9, ABW, NG, G2SW, G2SA, 18, true, U_LD, L_, GRP>(a.u + (size_t)d0 * U_LD + ks * 1152, a.w2 + (size_t)ks * 1152 * L_ + s0, smem, w4, lane, ab0, acc);
  pair_reduce<ABW, GRP, 10240>(acc, smem, w4, lane);
  const int fq = lane >> 4, c = lane & 15, ww = w4 & 1, wid8 = GRP * 4 + w4;
  const __amdgpu_buffer_rsrc_t rs = __builtin_amdgcn_make_buffer_rsrc((void*)a.slab, 0, (int)SLAB_BYTES, 0x00020000);
  constexpr int NE = 2 * ABW;
#pragma unroll
  for (int e = 0; e < NE; ++e) {
    const int owner = e & 3;
    if (owner != ks) {
      const int off = ((((tile * 4 + owner) * 4 + ks) * 8 + wid8) * 3 + (e >> 2)) * 1024 + lane * 16;
      __builtin_amdgcn_raw_buffer_store_b128(__builtin_bit_cast(u32x4, acc[e >> 1][2 * GRP + (e & 1)]), rs, off, 0, 16);
    }
  }
  asm volatile("s_waitcnt vmcnt(0)" ::: "memory");
  __syncthreads();
  if (wid8 == 0) {
    if (lane == 0) __hip_atomic_fetch_add(a.cnt + tile * 64, 1u, __ATOMIC_RELAXED, __HIP_MEMORY_SCOPE_AGENT);
    unsigned spins = 0;
    while ((unsigned)__builtin_amdgcn_readfirstlane(__hip_atomic_load(a.cnt + tile * 64, __ATOMIC_RELAXED, __HIP_MEMORY_SCOPE_AGENT)) < 4u) {
      __builtin_amdgcn_s_sleep(1);
      if (++spins > (1u << 24)) break;
    }
  }
  asm volatile("" ::: "memory");
  __syncthreads();
  asm volatile("" ::: "memory");
  constexpr int NSL = (NE + 3) / 4;
  u32x4 rv[NSL][3];
#pragma unroll
  for (int sl = 0; sl < NSL; ++sl)
#pragma unroll
    for (int k = 1; k < 4; ++k) {
      const int src = (ks + k) & 3;
      const int slc = (ks + 4 * sl < NE) ? sl : 0;
      const int off = ((((tile * 4 + ks) * 4 + src) * 8 + wid8) * 3 + slc) * 1024 + lane * 16;
      rv[sl][k - 1] = __builtin_amdgcn_raw_buffer_load_b128(rs, off, 0, 16);
    }
#pragma unroll
  for (int sl = 0; sl < NSL; ++sl) {
    const int e0 = 4 * sl;
    if (ks + e0 < NE) {
      f32x4 v = {0, 0, 0, 0};
#pragma unroll
      for (int k = 0; k < 4; ++k) if (k == ks && e0 + k < NE) v = acc[(e0 + k) >> 1][2 * GRP + ((e0 + k) & 1)];
#pragma unroll
      for (int k = 0; k < 3; ++k) v += __builtin_bit_cast(f32x4, rv[sl][k]);
      const int e = ks + e0;
      const int s = s0 + 64 * ww + 16 * (2 * GRP + (e & 1)) + c;
      const int d = d0 + 16 * (ab0 + (e >> 1)) + 4 * fq;
      const float g = gv[sl], omg = rbf(1.0f - g);
      float o[4];
#pragma unroll
      for (int j = 0; j < 4; ++j) o[j] = rbf(g * bf2f(avr[sl][j])) + rbf(omg * rbf(rbf(v[j]) + bf2f(bv2r[sl])));
      u32x2 ov = {pack2(o[0], o[1]), pack2(o[2], o[3])};
      *(u32x2*)(a.out + (size_t)s * D_ + d) = ov;
    }
  }
}
__global__ __launch_bounds__(512) void k3_gemm2(G2Args a) {
  extern __shared__ __attribute__((aligned(16))) char smem_[];
  LAS char* smem = (LAS char*)smem_;
  const int wid = __builtin_amdgcn_readfirstlane(threadIdx.x >> 6), lane = threadIdx.x & 63;
  const int b = blockIdx.x;
  const int id = (b & 7) * 32 + (b >> 3);
  const int st = id >> 4, ks = (id >> 2) & 3, dtile = id & 3;
  const int d0 = dtile * 144, s0 = st * 128, tile = st * 4 + dtile;
  switch (wid) {
    case 0: case 1: g2_body<5, 4, 0>(a, smem, wid, lane, d0, s0, ks, 0, tile); break;
    case 2: case 3: g2_body<4, 4, 0>(a, smem, wid, lane, d0, s0, ks, 5, tile); break;
    case 4: case 5: g2_body<4, 4, 1>(a, smem, wid - 2, lane, d0, s0, ks, 5, tile); break;
    default: g2_body<5, 5, 1>(a, smem, wid - 6, lane, d0, s0, ks, 0, tile); break;
  }
}

#define RUNK(k)
extern "C" void kernel_launch(void* const* d_in, const int* in_sizes, int n_in, void* d_out, int out_size, void* d_ws, size_t ws_size,
                              hipStream_t stream) {
  const bf16_t* x = (const bf16_t*)d_in[0];
  const bf16_t* logA = (const bf16_t*)d_in[1];
  const bf16_t* Aim = (const bf16_t*)d_in[2];
  const bf16_t* Cre = (const bf16_t*)d_in[3];
  const bf16_t* Cim = (const bf16_t*)d_in[4];
  const bf16_t* logdt = (const bf16_t*)d_in[5];
  const bf16_t* Dskip = (const bf16_t*)d_in[6];
  const bf16_t* w1 = (const bf16_t*)d_in[8];
  const bf16_t* b1 = (const bf16_t*)d_in[9];
  const bf16_t* w2 = (const bf16_t*)d_in[10];
  const bf16_t* b2 = (const bf16_t*)d_in[11];
  const bf16_t* gate_w = (const bf16_t*)d_in[12];
  char* ws = (char*)d_ws;
  bf16_t* hT = (bf16_t*)(ws + WS_HT);
  bf16_t* aT = (bf16_t*)(ws + WS_AT);
  float* gate = (float*)(ws + WS_G);
  bf16_t* u = (bf16_t*)(ws + WS_U);
  float* slab = (float*)(ws + WS_P);
  unsigned* cnt = (unsigned*)(ws + WS_CNT);
  (void)hipFuncSetAttribute((const void*)k1_prep, hipFuncAttributeMaxDynamicSharedMemorySize, K1_LDS);
  (void)hipFuncSetAttribute((const void*)k2_gemm1, hipFuncAttributeMaxDynamicSharedMemorySize, K2_LDS);
  (void)hipFuncSetAttribute((const void*)k3_gemm2, hipFuncAttributeMaxDynamicSharedMemorySize, G2_LDS);
  SsmArgs a1 = {x, logA, Aim, Cre, Cim, logdt, Dskip, gate_w, hT, aT, gate, w1, cnt, w2};
  SsmArgs a2 = {x, logA, Aim, Cre, Cim, logdt, Dskip, gate_w, hT, aT, gate, w2, cnt, w2};
  RUNK(0) k1_prep<<<256, 768, K1_LDS, stream>>>(a1);
  RUNK(1) k2_gemm1<<<216 + 32, 512, K2_LDS, stream>>>(hT, w1, b1, u, a2);
  G2Args g2 = {u, w2, slab, cnt, b2, gate, aT, (bf16_t*)d_out};
  RUNK(2) k3_gemm2<<<256, 512, G2_LDS, stream>>>(g2);
}
```
